```python
import torch

def _softmax(x, axis=-1):
    return torch.softmax(x, dim=axis)
def _gelu(x):
    return torch.nn.functional.gelu(x, approximate="tanh")
def _silu(x):
    return torch.nn.functional.silu(x)
def _sigmoid(x):
    return torch.sigmoid(x)
def _layernorm(x, axis=-1, eps=1e-5):
    m = x.mean(dim=axis, keepdim=True)
    v = x.var(dim=axis, keepdim=True, unbiased=False)
    return (x - m) / torch.sqrt(v + eps)
def _scatter_add(tgt, idx, src, axis=0):
    return tgt.clone().index_add_(axis, idx, src)
def _segment_sum(src, seg, n):
    out = torch.zeros((n,) + src.shape[1:], dtype=src.dtype)
    return out.index_add_(0, seg, src)
def _pad(x, pw):
    flat = tuple(v for pair in reversed(pw) for v in pair)
    return torch.nn.functional.pad(x, flat)

kernel_name = 'standalone__mha_block_cached__near_ridge'

def setup_inputs():

    _w = (lambda a: (lambda t: t.to(torch.bfloat16) if t.is_floating_point() else t)(torch.as_tensor(a)))
    B, Sq, D = 8, 128, 1024
    H, Sk, Dh = 32, 1024, 128
    return {
        "x": _w((lambda *s: torch.randn(*s, dtype=torch.bfloat16) * 0.8)(B, Sq, D)),
        "k_cache": _w((lambda *s: torch.randn(*s, dtype=torch.bfloat16) * 0.8)(B, H, Sk, Dh)),
        "v_cache": _w((lambda *s: torch.randn(*s, dtype=torch.bfloat16) * 0.05)(B, H, Sk, Dh)),
        "wq": _w((lambda *s: torch.randn(*s, dtype=torch.bfloat16) * 0.8)(D, H * Dh) * (D ** -0.5)),
        "wo": _w((lambda *s: torch.randn(*s, dtype=torch.bfloat16) * 0.05)(H * Dh, D) * ((H * Dh) ** -0.5)),
    }

def reference(x, k_cache, v_cache, wq, wo):
    B, Sq, D = x.shape
    H, Dh = 32, 128
    q = (lambda x, *d: x.permute(*d))((x @ wq).reshape(B, Sq, H, Dh), 0, 2, 1, 3)
    scores = torch.einsum("bhqd,bhkd->bhqk", q, k_cache) * (Dh ** -0.5)
    p = _softmax(scores, axis=-1)
    o = torch.einsum("bhqk,bhkd->bhqd", p, v_cache)
    return (lambda x, *d: x.permute(*d))(o, 0, 2, 1, 3).reshape(B, Sq, H * Dh) @ wo
```

```cpp
#include <hip/hip_runtime.h>
#include <hip/hip_bf16.h>

typedef __bf16   bf16x8 __attribute__((ext_vector_type(8)));
typedef __bf16   bf16x4 __attribute__((ext_vector_type(4)));
typedef __bf16   bf16x2 __attribute__((ext_vector_type(2)));
typedef float    f32x16 __attribute__((ext_vector_type(16)));
typedef float    f32x4  __attribute__((ext_vector_type(4)));
typedef float    f32x2  __attribute__((ext_vector_type(2)));
typedef unsigned u32x4  __attribute__((ext_vector_type(4)));
#define LAS __attribute__((address_space(3)))
#define DI __device__ __forceinline__
#define MFMA(a, b, c) __builtin_amdgcn_mfma_f32_32x32x16_bf16((a), (b), (c), 0, 0, 0)
#define WAITV(n) asm volatile("s_waitcnt vmcnt(" #n ")" ::: "memory")
#define BAR() do { asm volatile("s_waitcnt lgkmcnt(0)" ::: "memory"); __builtin_amdgcn_s_barrier(); asm volatile("" ::: "memory"); } while (0)

constexpr int B_ = 8, SQ = 128, DM = 1024, H_ = 32, SK = 1024, DH = 128;
constexpr unsigned SLOT = 32768u;
constexpr float QSCALE = 0.12751743082f;
constexpr float THR = 8.0f;

DI unsigned rfl(unsigned x) { return __builtin_amdgcn_readfirstlane(x); }
DI unsigned cvtpk(float lo, float hi) { f32x2 v = {lo, hi}; bf16x2 b = __builtin_convertvector(v, bf16x2); return __builtin_bit_cast(unsigned, b); }
DI bf16x8 pack8(const f32x16& x, int base) {
  u32x4 w = {cvtpk(x[base], x[base + 1]), cvtpk(x[base + 2], x[base + 3]), cvtpk(x[base + 4], x[base + 5]), cvtpk(x[base + 6], x[base + 7])};
  return __builtin_bit_cast(bf16x8, w);
}
template <int NT> DI void dma16(unsigned m0v, unsigned voff, __amdgpu_buffer_rsrc_t srd, unsigned soff) {
  if constexpr (NT) asm volatile("s_mov_b32 m0, %0\n\ts_nop 0\n\tbuffer_load_dwordx4 %1, %2, %3 offen nt lds" :: "s"(m0v), "v"(voff), "s"(srd), "s"(soff) : "memory");
  else              asm volatile("s_mov_b32 m0, %0\n\ts_nop 0\n\tbuffer_load_dwordx4 %1, %2, %3 offen lds" :: "s"(m0v), "v"(voff), "s"(srd), "s"(soff) : "memory");
}
DI bf16x8 ld128(unsigned addr) { return *(LAS bf16x8*)addr; }
DI bf16x8 ldtr(unsigned a0, unsigned a1) {
  bf16x4 lo = __builtin_amdgcn_ds_read_tr16_b64_v4bf16((LAS bf16x4*)a0);
  bf16x4 hi = __builtin_amdgcn_ds_read_tr16_b64_v4bf16((LAS bf16x4*)a1);
  return __builtin_shufflevector(lo, hi, 0, 1, 2, 3, 4, 5, 6, 7);
}
DI unsigned fsw(unsigned row) { return (((row >> 1) & 1u) << 2) | ((row >> 2) & 3u); }

__global__ __launch_bounds__(512, 2)
void k1_qattn(const char* __restrict__ x, const char* __restrict__ kc, const char* __restrict__ vc, const char* __restrict__ wq, char* __restrict__ ofrag) {
  extern __shared__ __attribute__((aligned(16))) char lds[];
  const unsigned tid = threadIdx.x, lane = tid & 63u, wid = rfl(tid >> 6), grp = wid >> 2, w4 = wid & 3u;
  const unsigned r = lane & 31u, hh = lane >> 5, blk = (lane >> 4) & 1u, q4 = (lane & 15u) >> 2, p4 = lane & 3u;
  const unsigned g = blockIdx.x & 7u, idx = blockIdx.x >> 3, h = 4u * g + (idx & 3u), b = idx >> 2;
  const unsigned ldsb = rfl((unsigned)(size_t)(LAS char*)lds);
  const __amdgpu_buffer_rsrc_t srdX = __builtin_amdgcn_make_buffer_rsrc((void*)(x + (size_t)b * (SQ * DM * 2)), (short)0, SQ * DM * 2, 0x00020000);
  const __amdgpu_buffer_rsrc_t srdW = __builtin_amdgcn_make_buffer_rsrc((void*)(wq + h * 256u), (short)0, (int)(DM * H_ * DH * 2 - h * 256u), 0x00020000);
  const __amdgpu_buffer_rsrc_t srdK = __builtin_amdgcn_make_buffer_rsrc((void*)(kc + (size_t)(b * H_ + h) * (SK * DH * 2)), (short)0, SK * DH * 2, 0x00020000);
  const __amdgpu_buffer_rsrc_t srdV = __builtin_amdgcn_make_buffer_rsrc((void*)(vc + (size_t)(b * H_ + h) * (SK * DH * 2)), (short)0, SK * DH * 2, 0x00020000);
  const unsigned rl = lane >> 3, ck0 = ((lane & 7u) ^ fsw(rl)) * 16u, ck1 = ck0 ^ 32u;
  const unsigned vkv0 = rl * 256u + ck0, vkv1 = rl * 256u + ck1;
  auto dma_kv = [&](unsigned t, unsigned slot) {
    const unsigned m = ldsb + slot * SLOT + w4 * 4096u, so = t * 16384u + w4 * 4096u;
    dma16<1>(m, vkv0, srdK, so);               dma16<1>(m + 1024u, vkv0, srdK, so + 128u);
    dma16<1>(m + 2048u, vkv1, srdK, so + 2048u); dma16<1>(m + 3072u, vkv1, srdK, so + 2176u);
    dma16<1>(m + 16384u, vkv0, srdV, so);               dma16<1>(m + 17408u, vkv0, srdV, so + 128u);
    dma16<1>(m + 18432u, vkv1, srdV, so + 2048u); dma16<1>(m + 19456u, vkv1, srdV, so + 2176u);
  };
  const unsigned trot = (blockIdx.x * 2u) & 15u;

  bf16x8 qf[8];
  const unsigned k0 = (2u * b + 4u * (h & 3u)) & 15u;
  if (grp == 1) {
    const unsigned vx0 = rl * 2048u + ck0, vx1 = rl * 2048u + ck1, vw0 = rl * 8192u + ck0, vw1 = rl * 8192u + ck1;
    auto dma_q = [&](unsigned i, unsigned slot) {
      const unsigned kt = (k0 + i) & 15u;
      const unsigned m = ldsb + slot * SLOT + w4 * 4096u, sx = w4 * 32u * 2048u + kt * 128u, sw = (kt * 64u + w4 * 16u) * 8192u;
      dma16<0>(m, vx0, srdX, sx);                 dma16<0>(m + 1024u, vx1, srdX, sx + 16384u);
      dma16<0>(m + 2048u, vx0, srdX, sx + 32768u); dma16<0>(m + 3072u, vx1, srdX, sx + 49152u);
      dma16<1>(m + 16384u, vw0, srdW, sw);                 dma16<1>(m + 17408u, vw0, srdW, sw + 128u);
      dma16<1>(m + 18432u, vw1, srdW, sw + 65536u); dma16<1>(m + 19456u, vw1, srdW, sw + 65664u);
    };
    dma_q(0, 2); dma_q(1, 0);
    WAITV(8); BAR();
    unsigned slot = 1;
    for (unsigned i = 0; i < 14; ++i) {
      dma_q(i + 2, slot); slot = slot == 2 ? 0 : slot + 1;
      WAITV(8); BAR();
    }
    dma_kv((trot + 1) & 15u, 4); WAITV(8); BAR();
    BAR();
  } else {
    f32x16 acc[4] = {};
    unsigned xb[4], wb[4];
#pragma unroll
    for (unsigned s = 0; s < 4; ++s) xb[s] = ldsb + 1024u * (4u * w4 + (r >> 3)) + 128u * (r & 7u) + 16u * ((2u * s + hh) ^ fsw(r));
#pragma unroll
    for (unsigned c = 0; c < 4; ++c) { const unsigned d1 = c >> 1, t = c & 1u;
      wb[c] = ldsb + 16384u + 2048u * hh + 128u * (4u * t + q4) + 16u * ((4u * d1 + 2u * blk + (p4 & 1u)) ^ ((((q4 >> 1) & 1u) << 2) | (2u * hh + t))) + 8u * (p4 >> 1); }
    BAR();
    unsigned slot = 2;
    for (unsigned i = 0; i < 16; ++i) {
      const unsigned so = slot * SLOT; slot = slot == 2 ? 0 : slot + 1;
      if (i == 14) dma_kv(trot, 3);
      if (i == 15) dma_kv((trot + 2) & 15u, 0);
#pragma unroll
      for (unsigned s = 0; s < 4; ++s) {
        const bf16x8 xf = ld128(xb[s] + so);
#pragma unroll
        for (unsigned db = 0; db < 4; ++db) {
          const unsigned im = so + 4096u * s + 1024u * (db >> 1);
          const bf16x8 wf = ldtr(wb[2 * (db & 1)] + im, wb[2 * (db & 1) + 1] + im);
          acc[db] = MFMA(wf, xf, acc[db]);
        }
      }
      BAR();
    }
#pragma unroll
    for (int db = 0; db < 4; ++db) {
#pragma unroll
      for (int e = 0; e < 16; ++e) acc[db][e] *= QSCALE;
      qf[2 * db] = pack8(acc[db], 0); qf[2 * db + 1] = pack8(acc[db], 8);
    }
#pragma unroll
    for (int s = 0; s < 8; ++s) *(LAS bf16x8*)(ldsb + 2u * SLOT + w4 * 8192u + s * 1024u + lane * 16u) = qf[s];
  }
  if (grp == 0) WAITV(8); else WAITV(0);
  BAR();
  if (grp == 1) {
#pragma unroll
    for (int s = 0; s < 8; ++s) qf[s] = ld128(ldsb + 2u * SLOT + w4 * 8192u + s * 1024u + lane * 16u);
  }
  BAR();

  unsigned kb4[4], vb4[4];
#pragma unroll
  for (unsigned s = 0; s < 4; ++s) kb4[s] = ldsb + 2048u * (r >> 3) + 128u * (r & 7u) + 16u * ((2u * s + hh) ^ fsw(r));
#pragma unroll
  for (unsigned c = 0; c < 4; ++c) { const unsigned d1 = c >> 1, t = c & 1u;
    vb4[c] = ldsb + 16384u + 2048u * t + 128u * (4u * hh + q4) + 16u * ((4u * d1 + 2u * blk + (p4 & 1u)) ^ ((((q4 >> 1) & 1u) << 2) | (2u * t + hh))) + 8u * (p4 >> 1); }
  f32x16 o[4] = {};
  f32x16 negm = {};
  float mref = 0.f, lsum = 0.f;
  if (grp == 0) __builtin_amdgcn_s_setprio(1);
  unsigned slot = 3u + grp;
  unsigned lslot = grp ? 1u : 2u;
  for (unsigned k = 0; k < 8; ++k) {
    const unsigned so = slot * SLOT; slot = slot >= 3 ? slot - 3 : slot + 2;
    if (grp == 1 && k < 7) dma_kv((trot + 2 * k + 3) & 15u, lslot);
    f32x16 s0, s1;
    {
      bf16x8 kf = ld128(kb4[0] + so); s0 = MFMA(kf, qf[0], negm);
      kf = ld128(kb4[0] + so + 8192u); s1 = MFMA(kf, qf[0], negm);
    }
#pragma unroll
    for (unsigned s = 1; s < 8; ++s) {
      bf16x8 kf = ld128(kb4[s & 3] + so + 1024u * (s >> 2)); s0 = MFMA(kf, qf[s], s0);
      kf = ld128(kb4[s & 3] + so + 8192u + 1024u * (s >> 2)); s1 = MFMA(kf, qf[s], s1);
    }
    float mx = fmaxf(s0[0], s1[0]);
#pragma unroll
    for (int e = 1; e < 16; ++e) mx = fmaxf(fmaxf(mx, s0[e]), s1[e]);
    if (k == 0 || __any(mx > THR)) {
      auto pr = __builtin_amdgcn_permlane32_swap(__float_as_uint(mx), __float_as_uint(mx), false, false);
      float dl = fmaxf(__uint_as_float(pr[0]), __uint_as_float(pr[1]));
      if (k != 0) {
        dl = fmaxf(dl, 0.f);
        const float f = __builtin_amdgcn_exp2f(-dl);
        lsum *= f;
#pragma unroll
        for (int db = 0; db < 4; ++db)
#pragma unroll
          for (int e = 0; e < 16; ++e) o[db][e] *= f;
      }
      mref += dl;
#pragma unroll
      for (int e = 0; e < 16; ++e) { s0[e] -= dl; s1[e] -= dl; negm[e] = -mref; }
    }
#pragma unroll
    for (int e = 0; e < 16; ++e) { s0[e] = __builtin_amdgcn_exp2f(s0[e]); s1[e] = __builtin_amdgcn_exp2f(s1[e]); }
    float a0 = 0.f, a1 = 0.f;
#pragma unroll
    for (int e = 0; e < 16; ++e) { a0 += s0[e]; a1 += s1[e]; }
    lsum += a0 + a1;
    bf16x8 pf[4] = {pack8(s0, 0), pack8(s0, 8), pack8(s1, 0), pack8(s1, 8)};
#pragma unroll
    for (unsigned ks = 0; ks < 4; ++ks)
#pragma unroll
      for (unsigned db = 0; db < 4; ++db) {
        const unsigned im = so + 4096u * ks + 1024u * (db >> 1);
        const bf16x8 vf = ldtr(vb4[2 * (db & 1)] + im, vb4[2 * (db & 1) + 1] + im);
        o[db] = MFMA(vf, pf[ks], o[db]);
      }
    if (grp == 0) { if (k < 6) { dma_kv((trot + 2 * k + 4) & 15u, lslot); WAITV(8); } else WAITV(0); }
    else WAITV(0);
    lslot = lslot >= 3 ? lslot - 3 : lslot + 2;
    BAR();
  }
  __builtin_amdgcn_s_setprio(0);
  {
    auto pr = __builtin_amdgcn_permlane32_swap(__float_as_uint(lsum), __float_as_uint(lsum), false, false);
    lsum = __uint_as_float(pr[0]) + __uint_as_float(pr[1]);
    *(LAS f32x2*)(ldsb + 4u * SLOT + (wid * 64u + lane) * 8u) = f32x2{mref, lsum};
    BAR();
    const f32x2 ot = *(LAS f32x2*)(ldsb + 4u * SLOT + ((wid ^ 4u) * 64u + lane) * 8u);
    const float M = fmaxf(mref, ot[0]);
    const float wme = __builtin_amdgcn_exp2f(mref - M), wot = __builtin_amdgcn_exp2f(ot[0] - M);
    const float sc = wme / (wme * lsum + wot * ot[1]);
#pragma unroll
    for (int db = 0; db < 4; ++db)
#pragma unroll
      for (int e = 0; e < 16; ++e) o[db][e] *= sc;
    const unsigned sb = ldsb + wid * 8192u + lane * 16u, rb = ldsb + (wid ^ 4u) * 8192u + lane * 16u;
    f32x16 ka, kb_;
    if (grp == 0) {
#pragma unroll
      for (int j = 0; j < 4; ++j) { *(LAS f32x4*)(sb + j * 1024u) = f32x4{o[2][4*j], o[2][4*j+1], o[2][4*j+2], o[2][4*j+3]};
                                    *(LAS f32x4*)(sb + 4096u + j * 1024u) = f32x4{o[3][4*j], o[3][4*j+1], o[3][4*j+2], o[3][4*j+3]}; }
      ka = o[0]; kb_ = o[1];
    } else {
#pragma unroll
      for (int j = 0; j < 4; ++j) { *(LAS f32x4*)(sb + j * 1024u) = f32x4{o[0][4*j], o[0][4*j+1], o[0][4*j+2], o[0][4*j+3]};
                                    *(LAS f32x4*)(sb + 4096u + j * 1024u) = f32x4{o[1][4*j], o[1][4*j+1], o[1][4*j+2], o[1][4*j+3]}; }
      ka = o[2]; kb_ = o[3];
    }
    BAR();
#pragma unroll
    for (int j = 0; j < 4; ++j) {
      const f32x4 u = *(LAS f32x4*)(rb + j * 1024u), v = *(LAS f32x4*)(rb + 4096u + j * 1024u);
#pragma unroll
      for (int e = 0; e < 4; ++e) { ka[4*j+e] += u[e]; kb_[4*j+e] += v[e]; }
    }
    char* op = ofrag + ((size_t)((b * 4u + w4) * 256u + h * 8u + grp * 4u) * 64u + lane) * 16u;
    *(bf16x8*)(op) = pack8(ka, 0); *(bf16x8*)(op + 1024) = pack8(ka, 8);
    *(bf16x8*)(op + 2048) = pack8(kb_, 0); *(bf16x8*)(op + 3072) = pack8(kb_, 8);
  }
}

__global__ __launch_bounds__(512, 2)
void k2_oproj(const char* __restrict__ ofrag, const char* __restrict__ wo, char* __restrict__ out) {
  extern __shared__ __attribute__((aligned(16))) char lds[];
  const unsigned tid = threadIdx.x, lane = tid & 63u, wid = rfl(tid >> 6);
  const unsigned hh = lane >> 5, blk = (lane >> 4) & 1u, q4 = (lane & 15u) >> 2, p4 = lane & 3u;
  const unsigned g = blockIdx.x & 7u, idx = blockIdx.x >> 3, mt = 8u * (g & 1u) + (idx & 7u), nt = 4u * (g >> 1) + (idx >> 3);
  const unsigned ldsb = rfl((unsigned)(size_t)(LAS char*)lds) + wid * 16384u;
  const __amdgpu_buffer_rsrc_t srdA = __builtin_amdgcn_make_buffer_rsrc((void*)(ofrag + (size_t)mt * 524288u), (short)0, 524288, 0x00020000);
  const __amdgpu_buffer_rsrc_t srdB = __builtin_amdgcn_make_buffer_rsrc((void*)(wo + nt * 128u), (short)0, (int)(4096u * 2048u - nt * 128u), 0x00020000);
  const unsigned rl = lane >> 3, ck0 = ((lane & 7u) ^ fsw(rl)) * 16u;
  const unsigned vb0 = rl * 2048u + ck0, vb1 = rl * 2048u + (ck0 ^ 32u), va = lane * 16u;
  const unsigned rot = (4u * (idx & 7u) + 8u * (idx >> 3)) & 31u;
  auto issue = [&](unsigned i) {
    const unsigned ks = wid * 32u + ((i + rot) & 31u), m = ldsb + (i & 3u) * 4096u;
    dma16<0>(m, va, srdA, ks * 1024u); dma16<0>(m + 1024u, va, srdA, 262144u + ks * 1024u);
    dma16<1>(m + 2048u, vb0, srdB, ks * 32768u); dma16<1>(m + 3072u, vb1, srdB, ks * 32768u + 16384u);
  };
  unsigned bb[4];
#pragma unroll
  for (unsigned c = 0; c < 4; ++c) { const unsigned nb = c >> 1, t = c & 1u;
    bb[c] = ldsb + 2048u + 1024u * hh + 128u * (4u * t + q4) + 16u * ((4u * nb + 2u * blk + (p4 >> 1)) ^ ((((q4 >> 1) & 1u) << 2) | (2u * hh + t))) + 8u * (p4 & 1u); }
  f32x16 acc[2][2] = {};
  issue(0); issue(1); issue(2);
  for (unsigned i = 0; i < 32; ++i) {
    if (i + 3 < 32) { issue(i + 3); WAITV(12); } else if (i == 29) WAITV(8); else if (i == 30) WAITV(4); else WAITV(0);
    const unsigned so = (i & 3u) * 4096u;
    const bf16x8 a0 = ld128(ldsb + so + va), a1 = ld128(ldsb + so + 1024u + va);
    const bf16x8 b0 = ldtr(bb[0] + so, bb[1] + so), b1 = ldtr(bb[2] + so, bb[3] + so);
    acc[0][0] = MFMA(b0, a0, acc[0][0]); acc[0][1] = MFMA(b0, a1, acc[0][1]);
    acc[1][0] = MFMA(b1, a0, acc[1][0]); acc[1][1] = MFMA(b1, a1, acc[1][1]);
    asm volatile("s_waitcnt lgkmcnt(0)" ::: "memory");
  }
#pragma unroll
  for (int nb = 0; nb < 2; ++nb)
#pragma unroll
    for (int qb = 0; qb < 2; ++qb)
#pragma unroll
      for (int j = 0; j < 4; ++j)
        *(LAS f32x4*)(ldsb + ((nb * 2 + qb) * 4 + j) * 1024u + lane * 16u) = f32x4{acc[nb][qb][4*j], acc[nb][qb][4*j+1], acc[nb][qb][4*j+2], acc[nb][qb][4*j+3]};
  BAR();
  {
    const unsigned qb = wid & 1u, n8 = (wid >> 1) * 2u + hh, nb = n8 >> 2, j = n8 & 3u;
    const unsigned base = rfl((unsigned)(size_t)(LAS char*)lds) + ((nb * 2u + qb) * 4u + j) * 1024u + (lane & 31u) * 16u;
    f32x4 lo = {0.f, 0.f, 0.f, 0.f}, hi = lo;
#pragma unroll
    for (unsigned w = 0; w < 8; ++w) { lo += *(LAS f32x4*)(base + w * 16384u); hi += *(LAS f32x4*)(base + w * 16384u + 512u); }
    u32x4 o = {cvtpk(lo[0], lo[1]), cvtpk(lo[2], lo[3]), cvtpk(hi[0], hi[1]), cvtpk(hi[2], hi[3])};
    *(u32x4*)(out + (size_t)(mt * 64u + qb * 32u + (lane & 31u)) * 2048u + nt * 128u + n8 * 16u) = o;
  }
}

extern "C" void kernel_launch(void* const* d_in, const int* in_sizes, int n_in, void* d_out, int out_size, void* d_ws, size_t ws_size, hipStream_t stream) {
  (void)in_sizes; (void)n_in; (void)out_size; (void)ws_size;
  hipFuncSetAttribute((const void*)k1_qattn, hipFuncAttributeMaxDynamicSharedMemorySize, 163840);
  hipFuncSetAttribute((const void*)k2_oproj, hipFuncAttributeMaxDynamicSharedMemorySize, 131072);
  k1_qattn<<<256, 512, 163840, stream>>>((const char*)d_in[0], (const char*)d_in[1], (const char*)d_in[2], (const char*)d_in[3], (char*)d_ws);
  k2_oproj<<<256, 512, 131072, stream>>>((const char*)d_ws, (const char*)d_in[4], (char*)d_out);
}
```

```cpp
#include <hip/hip_runtime.h>
#include <hip/hip_bf16.h>

typedef __bf16   bf16x8 __attribute__((ext_vector_type(8)));
typedef __bf16   bf16x4 __attribute__((ext_vector_type(4)));
typedef __bf16   bf16x2 __attribute__((ext_vector_type(2)));
typedef float    f32x16 __attribute__((ext_vector_type(16)));
typedef float    f32x4  __attribute__((ext_vector_type(4)));
typedef float    f32x2  __attribute__((ext_vector_type(2)));
typedef unsigned u32x4  __attribute__((ext_vector_type(4)));
#define LAS __attribute__((address_space(3)))
#define DI __device__ __forceinline__
#define MFMA(a, b, c) __builtin_amdgcn_mfma_f32_32x32x16_bf16((a), (b), (c), 0, 0, 0)
#define WAITV(n) asm volatile("s_waitcnt vmcnt(" #n ")" ::: "memory")
#define BAR() do { asm volatile("s_waitcnt lgkmcnt(0)" ::: "memory"); __builtin_amdgcn_s_barrier(); asm volatile("" ::: "memory"); } while (0)

#define SB() __builtin_amdgcn_sched_barrier(0)
#define OPAQUE(x) asm volatile("" : "+v"(x))
#define BARX() do { asm volatile("" ::: "memory"); __builtin_amdgcn_s_barrier(); asm volatile("" ::: "memory"); } while (0)
constexpr int B_ = 8, SQ = 128, DM = 1024, H_ = 32, SK = 1024, DH = 128;
constexpr unsigned SLOT = 32768u;
constexpr float QSCALE = 0.12751743082f;
constexpr float THR = 8.0f;

DI unsigned rfl(unsigned x) { return __builtin_amdgcn_readfirstlane(x); }
DI unsigned cvtpk(float lo, float hi) { f32x2 v = {lo, hi}; bf16x2 b = __builtin_convertvector(v, bf16x2); return __builtin_bit_cast(unsigned, b); }
DI bf16x8 pack8(const f32x16& x, int base) {
  u32x4 w = {cvtpk(x[base], x[base + 1]), cvtpk(x[base + 2], x[base + 3]), cvtpk(x[base + 4], x[base + 5]), cvtpk(x[base + 6], x[base + 7])};
  return __builtin_bit_cast(bf16x8, w);
}
template <int NT> DI void dma16(unsigned m0v, unsigned voff, __amdgpu_buffer_rsrc_t srd, unsigned soff) {
  if constexpr (NT) asm volatile("s_mov_b32 m0, %0\n\ts_nop 0\n\tbuffer_load_dwordx4 %1, %2, %3 offen nt lds" :: "s"(m0v), "v"(voff), "s"(srd), "s"(soff) : "memory");
  else              asm volatile("s_mov_b32 m0, %0\n\ts_nop 0\n\tbuffer_load_dwordx4 %1, %2, %3 offen lds" :: "s"(m0v), "v"(voff), "s"(srd), "s"(soff) : "memory");
}
DI bf16x8 ld128(unsigned addr) { return *(LAS bf16x8*)addr; }
DI bf16x8 ldtr(unsigned a0, unsigned a1) {
  bf16x4 lo = __builtin_amdgcn_ds_read_tr16_b64_v4bf16((LAS bf16x4*)a0);
  bf16x4 hi = __builtin_amdgcn_ds_read_tr16_b64_v4bf16((LAS bf16x4*)a1);
  return __builtin_shufflevector(lo, hi, 0, 1, 2, 3, 4, 5, 6, 7);
}
DI unsigned fsw(unsigned row) { return (((row >> 1) & 1u) << 2) | ((row >> 2) & 3u); }

__global__ __launch_bounds__(512, 2)
void k1_qattn(const char* __restrict__ x, const char* __restrict__ kc, const char* __restrict__ vc, const char* __restrict__ wq, char* __restrict__ ofrag) {
  extern __shared__ __attribute__((aligned(16))) char lds[];
  const unsigned tid = threadIdx.x, lane = tid & 63u, wid = rfl(tid >> 6), grp = wid >> 2, w4 = wid & 3u;
  const unsigned r = lane & 31u, hh = lane >> 5, blk = (lane >> 4) & 1u, q4 = (lane & 15u) >> 2, p4 = lane & 3u;
  const unsigned g = blockIdx.x & 7u, idx = blockIdx.x >> 3, h = 4u * g + (idx & 3u), b = idx >> 2;
  const unsigned ldsb = rfl((unsigned)(size_t)(LAS char*)lds);
  const __amdgpu_buffer_rsrc_t srdX = __builtin_amdgcn_make_buffer_rsrc((void*)(x + (size_t)b * (SQ * DM * 2)), (short)0, SQ * DM * 2, 0x00020000);
  const __amdgpu_buffer_rsrc_t srdW = __builtin_amdgcn_make_buffer_rsrc((void*)(wq + h * 256u), (short)0, (int)(DM * H_ * DH * 2 - h * 256u), 0x00020000);
  const __amdgpu_buffer_rsrc_t srdK = __builtin_amdgcn_make_buffer_rsrc((void*)(kc + (size_t)(b * H_ + h) * (SK * DH * 2)), (short)0, SK * DH * 2, 0x00020000);
  const __amdgpu_buffer_rsrc_t srdV = __builtin_amdgcn_make_buffer_rsrc((void*)(vc + (size_t)(b * H_ + h) * (SK * DH * 2)), (short)0, SK * DH * 2, 0x00020000);
  const unsigned rl = lane >> 3, ck0 = ((lane & 7u) ^ fsw(rl)) * 16u, ck1 = ck0 ^ 32u;
  const unsigned vkv0 = rl * 256u + ck0, vkv1 = rl * 256u + ck1;
  auto dma_kv = [&](unsigned t, unsigned slot) {
    const unsigned m = ldsb + slot * SLOT + w4 * 4096u, so = t * 16384u + w4 * 4096u;
    dma16<1>(m, vkv0, srdK, so);               dma16<1>(m + 1024u, vkv0, srdK, so + 128u);
    dma16<1>(m + 2048u, vkv1, srdK, so + 2048u); dma16<1>(m + 3072u, vkv1, srdK, so + 2176u);
    dma16<1>(m + 16384u, vkv0, srdV, so);               dma16<1>(m + 17408u, vkv0, srdV, so + 128u);
    dma16<1>(m + 18432u, vkv1, srdV, so + 2048u); dma16<1>(m + 19456u, vkv1, srdV, so + 2176u);
  };
  const unsigned trot = 0u;

  bf16x8 qf[8];
  const unsigned k0 = (2u * b + 4u * (h & 3u)) & 15u;
  if (grp == 1) {
    const unsigned vx0 = rl * 2048u + ck0, vx1 = rl * 2048u + ck1, vw0 = rl * 8192u + ck0, vw1 = rl * 8192u + ck1;
    auto dma_q = [&](unsigned i, unsigned slot) {
      const unsigned kt = (k0 + i) & 15u;
      const unsigned m = ldsb + slot * SLOT + w4 * 4096u, sx = w4 * 32u * 2048u + kt * 128u, sw = (kt * 64u + w4 * 16u) * 8192u;
      dma16<0>(m, vx0, srdX, sx);                 dma16<0>(m + 1024u, vx1, srdX, sx + 16384u);
      dma16<0>(m + 2048u, vx0, srdX, sx + 32768u); dma16<0>(m + 3072u, vx1, srdX, sx + 49152u);
      dma16<1>(m + 16384u, vw0, srdW, sw);                 dma16<1>(m + 17408u, vw0, srdW, sw + 128u);
      dma16<1>(m + 18432u, vw1, srdW, sw + 65536u); dma16<1>(m + 19456u, vw1, srdW, sw + 65664u);
    };
    dma_q(0, 3); dma_q(1, 0); dma_q(2, 1);
    WAITV(8); BAR();
    for (unsigned i = 0; i < 13; ++i) {
      dma_q(i + 3, (i + 6) & 3u);
      WAITV(8); BAR();
    }
    dma_kv((trot + 1) & 15u, 4); WAITV(8); BAR();
    BAR();
    BAR();
  } else {
    f32x16 acc[4] = {};
    unsigned xb[4], wb[4];
#pragma unroll
    for (unsigned s = 0; s < 4; ++s) xb[s] = ldsb + 1024u * (4u * w4 + (r >> 3)) + 128u * (r & 7u) + 16u * ((2u * s + hh) ^ fsw(r));
#pragma unroll
    for (unsigned c = 0; c < 4; ++c) { const unsigned d1 = c >> 1, t = c & 1u;
      wb[c] = ldsb + 16384u + 2048u * hh + 128u * (4u * t + q4) + 16u * ((4u * d1 + 2u * blk + (p4 & 1u)) ^ ((((q4 >> 1) & 1u) << 2) | (2u * hh + t))) + 8u * (p4 >> 1); }
    BAR();
    bf16x8 xf[2], wf[2][4];
    unsigned xa[4], wa[4];
#pragma unroll
    for (int c = 0; c < 4; ++c) { xa[c] = xb[c] + 3u * SLOT; wa[c] = wb[c] + 3u * SLOT; }
    xf[0] = ld128(xa[0]);
#pragma unroll
    for (unsigned db = 0; db < 4; ++db) wf[0][db] = ldtr(wa[2 * (db & 1)] + 1024u * (db >> 1), wa[2 * (db & 1) + 1] + 1024u * (db >> 1));
    for (unsigned i = 0; i < 16; ++i) {
      if (i == 13) dma_kv(trot, 3);
      if (i == 15) dma_kv((trot + 2) & 15u, 0);
      const unsigned nso = ((i + 4u) & 3u) * SLOT;
      SB();
#pragma unroll
      for (unsigned s = 0; s < 4; ++s) {
        const unsigned cur = s & 1, nxt = cur ^ 1;
        if (s == 3) {
#pragma unroll
          for (int c = 0; c < 4; ++c) { xa[c] = xb[c] + nso; wa[c] = wb[c] + nso; OPAQUE(xa[c]); OPAQUE(wa[c]); }
        }
        const unsigned sn = (s + 1) & 3;
#pragma unroll
        for (unsigned db = 0; db < 4; ++db) {
          acc[db] = MFMA(wf[cur][db], xf[cur], acc[db]);
          if (db == 0) xf[nxt] = ld128(xa[sn]);
          const unsigned im = 4096u * sn + 1024u * (db >> 1);
          wf[nxt][db] = ldtr(wa[2 * (db & 1)] + im, wa[2 * (db & 1) + 1] + im);
          SB();
        }
      }
      BARX();
    }
#pragma unroll
    for (int db = 0; db < 4; ++db) {
#pragma unroll
      for (int e = 0; e < 16; ++e) acc[db][e] *= QSCALE;
      qf[2 * db] = pack8(acc[db], 0); qf[2 * db + 1] = pack8(acc[db], 8);
    }
#pragma unroll
    for (int s = 0; s < 8; ++s) *(LAS bf16x8*)(ldsb + 2u * SLOT + w4 * 8192u + s * 1024u + lane * 16u) = qf[s];
  }
  if (grp == 0) WAITV(8); else WAITV(0);
  BAR();
  if (grp == 1) {
#pragma unroll
    for (int s = 0; s < 8; ++s) qf[s] = ld128(ldsb + 2u * SLOT + w4 * 8192u + s * 1024u + lane * 16u);
  }
  BAR();

  unsigned kb4[4], vb4[4];
#pragma unroll
  for (unsigned s = 0; s < 4; ++s) kb4[s] = ldsb + 2048u * (r >> 3) + 128u * (r & 7u) + 16u * ((2u * s + hh) ^ fsw(r));
#pragma unroll
  for (unsigned c = 0; c < 4; ++c) { const unsigned d1 = c >> 1, t = c & 1u;
    vb4[c] = ldsb + 16384u + 2048u * t + 128u * (4u * hh + q4) + 16u * ((4u * d1 + 2u * blk + (p4 & 1u)) ^ ((((q4 >> 1) & 1u) << 2) | (2u * t + hh))) + 8u * (p4 >> 1); }
  f32x16 o[4] = {};
  f32x16 negm = {};
  float mref = 0.f, lsum = 0.f, lsum2 = 0.f;
  if (grp == 0) __builtin_amdgcn_s_setprio(1);
  unsigned slot = 3u + grp;
  unsigned lslot = grp ? 1u : 2u;
  for (unsigned k = 0; k < 8; ++k) {
    const unsigned so = slot * SLOT; slot = slot >= 3 ? slot - 3 : slot + 2;
    if (grp == 1 && k < 7) dma_kv((trot + 2 * k + 3) & 15u, lslot);
    unsigned ka[4], va[4];
#pragma unroll
    for (int c = 0; c < 4; ++c) { ka[c] = kb4[c] + so; va[c] = vb4[c] + so; OPAQUE(ka[c]); OPAQUE(va[c]); }
    f32x16 s0, s1;
    bf16x8 kf[16], vf[16];
#define KRD(m) kf[m] = ld128(ka[(m) & 3] + 8192u * ((m) >> 3) + 1024u * (((m) >> 2) & 1))
#define VRD(m) vf[m] = ldtr(va[2 * ((m) & 1)] + 4096u * ((m) >> 2) + 1024u * (((m) >> 1) & 1), va[2 * ((m) & 1) + 1] + 4096u * ((m) >> 2) + 1024u * (((m) >> 1) & 1))
    KRD(0); KRD(1); KRD(2); KRD(3); KRD(4);
    SB();
    float mx;
#pragma unroll
    for (int m = 0; m < 16; ++m) {
      if (m == 0) s0 = MFMA(kf[0], qf[0], negm);
      else if (m < 8) s0 = MFMA(kf[m], qf[m], s0);
      else if (m == 8) s1 = MFMA(kf[8], qf[0], negm);
      else s1 = MFMA(kf[m], qf[m - 8], s1);
      if (m + 5 < 16) KRD(m + 5);
      if (m >= 12) VRD(m - 12);
      if (m == 10) mx = fmaxf(fmaxf(s0[0], s0[1]), s0[2]);
      if (m > 10) { mx = fmaxf(fmaxf(mx, s0[2 * m - 19]), s0[2 * m - 18]); }
      if (m == 15) { mx = fmaxf(fmaxf(mx, s0[13]), s0[14]); mx = fmaxf(mx, s0[15]); }
      SB();
    }
#pragma unroll
    for (int e = 0; e < 16; e += 2) mx = fmaxf(fmaxf(mx, s1[e]), s1[e + 1]);
    if (k == 0 || __any(mx > THR)) {
      auto pr = __builtin_amdgcn_permlane32_swap(__float_as_uint(mx), __float_as_uint(mx), false, false);
      float dl = fmaxf(__uint_as_float(pr[0]), __uint_as_float(pr[1]));
      if (k != 0) {
        dl = fmaxf(dl, 0.f);
        const float f = __builtin_amdgcn_exp2f(-dl);
        lsum *= f; lsum2 *= f;
#pragma unroll
        for (int db = 0; db < 4; ++db)
#pragma unroll
          for (int e = 0; e < 16; ++e) o[db][e] *= f;
      }
      mref += dl;
#pragma unroll
      for (int e = 0; e < 16; ++e) { s0[e] -= dl; s1[e] -= dl; negm[e] = -mref; }
    }
    bf16x8 pf[4];
#pragma unroll
    for (int e = 0; e < 8; ++e) s0[e] = __builtin_amdgcn_exp2f(s0[e]);
    pf[0] = pack8(s0, 0);
    SB();
#pragma unroll
    for (int m = 0; m < 16; ++m) {
      const int ks = m >> 2, db = m & 3;
      o[db] = MFMA(vf[m], pf[ks], o[db]);
      if (m + 4 < 16) VRD(m + 4);
      if (ks == 0) { s0[8 + 2 * db] = __builtin_amdgcn_exp2f(s0[8 + 2 * db]); s0[9 + 2 * db] = __builtin_amdgcn_exp2f(s0[9 + 2 * db]); lsum += s0[2 * db]; lsum2 += s0[2 * db + 1]; if (db == 3) pf[1] = pack8(s0, 8); }
      if (ks == 1) { s1[2 * db] = __builtin_amdgcn_exp2f(s1[2 * db]); s1[1 + 2 * db] = __builtin_amdgcn_exp2f(s1[1 + 2 * db]); lsum += s0[8 + 2 * db]; lsum2 += s0[9 + 2 * db]; if (db == 3) pf[2] = pack8(s1, 0); }
      if (ks == 2) { s1[8 + 2 * db] = __builtin_amdgcn_exp2f(s1[8 + 2 * db]); s1[9 + 2 * db] = __builtin_amdgcn_exp2f(s1[9 + 2 * db]); lsum += s1[2 * db]; lsum2 += s1[2 * db + 1]; if (db == 3) pf[3] = pack8(s1, 8); }
      if (ks == 3) { lsum += s1[8 + 2 * db]; lsum2 += s1[9 + 2 * db]; }
      SB();
    }
    if (grp == 0) { if (k < 6) { dma_kv((trot + 2 * k + 4) & 15u, lslot); WAITV(8); } else WAITV(0); }
    else WAITV(0);
    lslot = lslot >= 3 ? lslot - 3 : lslot + 2;
    BAR();
  }
  __builtin_amdgcn_s_setprio(0);
  {
    lsum += lsum2;
    auto pr = __builtin_amdgcn_permlane32_swap(__float_as_uint(lsum), __float_as_uint(lsum), false, false);
    lsum = __uint_as_float(pr[0]) + __uint_as_float(pr[1]);
    *(LAS f32x2*)(ldsb + 4u * SLOT + (wid * 64u + lane) * 8u) = f32x2{mref, lsum};
    BAR();
    const f32x2 ot = *(LAS f32x2*)(ldsb + 4u * SLOT + ((wid ^ 4u) * 64u + lane) * 8u);
    const float M = fmaxf(mref, ot[0]);
    const float wme = __builtin_amdgcn_exp2f(mref - M), wot = __builtin_amdgcn_exp2f(ot[0] - M);
    const float sc = wme / (wme * lsum + wot * ot[1]);
#pragma unroll
    for (int db = 0; db < 4; ++db)
#pragma unroll
      for (int e = 0; e < 16; ++e) o[db][e] *= sc;
    const unsigned sb = ldsb + wid * 8192u + lane * 16u, rb = ldsb + (wid ^ 4u) * 8192u + lane * 16u;
    f32x16 ka, kb_;
    if (grp == 0) {
#pragma unroll
      for (int j = 0; j < 4; ++j) { *(LAS f32x4*)(sb + j * 1024u) = f32x4{o[2][4*j], o[2][4*j+1], o[2][4*j+2], o[2][4*j+3]};
                                    *(LAS f32x4*)(sb + 4096u + j * 1024u) = f32x4{o[3][4*j], o[3][4*j+1], o[3][4*j+2], o[3][4*j+3]}; }
      ka = o[0]; kb_ = o[1];
    } else {
#pragma unroll
      for (int j = 0; j < 4; ++j) { *(LAS f32x4*)(sb + j * 1024u) = f32x4{o[0][4*j], o[0][4*j+1], o[0][4*j+2], o[0][4*j+3]};
                                    *(LAS f32x4*)(sb + 4096u + j * 1024u) = f32x4{o[1][4*j], o[1][4*j+1], o[1][4*j+2], o[1][4*j+3]}; }
      ka = o[2]; kb_ = o[3];
    }
    BAR();
#pragma unroll
    for (int j = 0; j < 4; ++j) {
      const f32x4 u = *(LAS f32x4*)(rb + j * 1024u), v = *(LAS f32x4*)(rb + 4096u + j * 1024u);
#pragma unroll
      for (int e = 0; e < 4; ++e) { ka[4*j+e] += u[e]; kb_[4*j+e] += v[e]; }
    }
    char* op = ofrag + ((size_t)((b * 4u + w4) * 256u + h * 8u + grp * 4u) * 64u + lane) * 16u;
    *(bf16x8*)(op) = pack8(ka, 0); *(bf16x8*)(op + 1024) = pack8(ka, 8);
    *(bf16x8*)(op + 2048) = pack8(kb_, 0); *(bf16x8*)(op + 3072) = pack8(kb_, 8);
  }
}

__global__ __launch_bounds__(512, 2)
void k2_oproj(const char* __restrict__ ofrag, const char* __restrict__ wo, char* __restrict__ out) {
  extern __shared__ __attribute__((aligned(16))) char lds[];
  const unsigned tid = threadIdx.x, lane = tid & 63u, wid = rfl(tid >> 6);
  const unsigned hh = lane >> 5, blk = (lane >> 4) & 1u, q4 = (lane & 15u) >> 2, p4 = lane & 3u;
  const unsigned g = blockIdx.x & 7u, idx = blockIdx.x >> 3, mt = 8u * (g & 1u) + (idx & 7u), nt = 4u * (g >> 1) + (idx >> 3);
  const unsigned ldsb = rfl((unsigned)(size_t)(LAS char*)lds) + wid * 16384u;
  const __amdgpu_buffer_rsrc_t srdA = __builtin_amdgcn_make_buffer_rsrc((void*)(ofrag + (size_t)mt * 524288u), (short)0, 524288, 0x00020000);
  const __amdgpu_buffer_rsrc_t srdB = __builtin_amdgcn_make_buffer_rsrc((void*)(wo + nt * 128u), (short)0, (int)(4096u * 2048u - nt * 128u), 0x00020000);
  const unsigned rl = lane >> 3, ck0 = ((lane & 7u) ^ fsw(rl)) * 16u;
  const unsigned vb0 = rl * 2048u + ck0, vb1 = rl * 2048u + (ck0 ^ 32u), va = lane * 16u;
  const unsigned rot = (4u * (idx & 7u) + 8u * (idx >> 3)) & 31u;
  auto issue = [&](unsigned i) {
    const unsigned ks = wid * 32u + ((i + rot) & 31u), m = ldsb + (i & 3u) * 4096u;
    dma16<0>(m, va, srdA, ks * 1024u); dma16<0>(m + 1024u, va, srdA, 262144u + ks * 1024u);
    dma16<1>(m + 2048u, vb0, srdB, ks * 32768u); dma16<1>(m + 3072u, vb1, srdB, ks * 32768u + 16384u);
  };
  unsigned bb[4];
#pragma unroll
  for (unsigned c = 0; c < 4; ++c) { const unsigned nb = c >> 1, t = c & 1u;
    bb[c] = ldsb + 2048u + 1024u * hh + 128u * (4u * t + q4) + 16u * ((4u * nb + 2u * blk + (p4 >> 1)) ^ ((((q4 >> 1) & 1u) << 2) | (2u * hh + t))) + 8u * (p4 & 1u); }
  f32x16 acc[2][2] = {};
  issue(0); issue(1); issue(2);
  for (unsigned i = 0; i < 32; ++i) {
    if (i + 3 < 32) { issue(i + 3); WAITV(12); } else if (i == 29) WAITV(8); else if (i == 30) WAITV(4); else WAITV(0);
    const unsigned so = (i & 3u) * 4096u;
    const bf16x8 a0 = ld128(ldsb + so + va), a1 = ld128(ldsb + so + 1024u + va);
    const bf16x8 b0 = ldtr(bb[0] + so, bb[1] + so), b1 = ldtr(bb[2] + so, bb[3] + so);
    acc[0][0] = MFMA(b0, a0, acc[0][0]); acc[0][1] = MFMA(b0, a1, acc[0][1]);
    acc[1][0] = MFMA(b1, a0, acc[1][0]); acc[1][1] = MFMA(b1, a1, acc[1][1]);
    asm volatile("s_waitcnt lgkmcnt(0)" ::: "memory");
  }
#pragma unroll
  for (int nb = 0; nb < 2; ++nb)
#pragma unroll
    for (int qb = 0; qb < 2; ++qb)
#pragma unroll
      for (int j = 0; j < 4; ++j)
        *(LAS f32x4*)(ldsb + ((nb * 2 + qb) * 4 + j) * 1024u + lane * 16u) = f32x4{acc[nb][qb][4*j], acc[nb][qb][4*j+1], acc[nb][qb][4*j+2], acc[nb][qb][4*j+3]};
  BAR();
  {
    const unsigned qb = wid & 1u, n8 = (wid >> 1) * 2u + hh, nb = n8 >> 2, j = n8 & 3u;
    const unsigned base = rfl((unsigned)(size_t)(LAS char*)lds) + ((nb * 2u + qb) * 4u + j) * 1024u + (lane & 31u) * 16u;
    f32x4 lo = {0.f, 0.f, 0.f, 0.f}, hi = lo;
#pragma unroll
    for (unsigned w = 0; w < 8; ++w) { lo += *(LAS f32x4*)(base + w * 16384u); hi += *(LAS f32x4*)(base + w * 16384u + 512u); }
    u32x4 o = {cvtpk(lo[0], lo[1]), cvtpk(lo[2], lo[3]), cvtpk(hi[0], hi[1]), cvtpk(hi[2], hi[3])};
    *(u32x4*)(out + (size_t)(mt * 64u + qb * 32u + (lane & 31u)) * 2048u + nt * 128u + n8 * 16u) = o;
  }
}

extern "C" void kernel_launch(void* const* d_in, const int* in_sizes, int n_in, void* d_out, int out_size, void* d_ws, size_t ws_size, hipStream_t stream) {
  (void)in_sizes; (void)n_in; (void)out_size; (void)ws_size;
  hipFuncSetAttribute((const void*)k1_qattn, hipFuncAttributeMaxDynamicSharedMemorySize, 163840);
  hipFuncSetAttribute((const void*)k2_oproj, hipFuncAttributeMaxDynamicSharedMemorySize, 131072);
  hipMemsetAsync((char*)d_ws + (32u << 20), 0, 16384, stream);
  k1_qattn<<<256, 512, 163840, stream>>>((const char*)d_in[0], (const char*)d_in[1], (const char*)d_in[2], (const char*)d_in[3], (char*)d_ws);
  k2_oproj<<<256, 512, 131072, stream>>>((const char*)d_ws, (const char*)d_in[4], (char*)d_out);
}
```

```cpp
#include <hip/hip_runtime.h>
#include <hip/hip_bf16.h>

typedef __bf16   bf16x8 __attribute__((ext_vector_type(8)));
typedef __bf16   bf16x4 __attribute__((ext_vector_type(4)));
typedef __bf16   bf16x2 __attribute__((ext_vector_type(2)));
typedef float    f32x16 __attribute__((ext_vector_type(16)));
typedef float    f32x4  __attribute__((ext_vector_type(4)));
typedef float    f32x2  __attribute__((ext_vector_type(2)));
typedef unsigned u32x4  __attribute__((ext_vector_type(4)));
typedef unsigned long long u64;
#define LAS __attribute__((address_space(3)))
#define GAS __attribute__((address_space(1)))
#define DI __device__ __forceinline__
#define MFMA(a, b, c) __builtin_amdgcn_mfma_f32_32x32x16_bf16((a), (b), (c), 0, 0, 0)
#define WAITV(n) asm volatile("s_waitcnt vmcnt(" #n ")" ::: "memory")
#define SB() __builtin_amdgcn_sched_barrier(0)
#define OPAQUE(x) asm volatile("" : "+v"(x))
#define BAR() do { asm volatile("s_waitcnt lgkmcnt(0)" ::: "memory"); __builtin_amdgcn_s_barrier(); asm volatile("" ::: "memory"); } while (0)
#define BARX() do { asm volatile("" ::: "memory"); __builtin_amdgcn_s_barrier(); asm volatile("" ::: "memory"); } while (0)
#define STAMP(i)

constexpr int SQ = 128, DM = 1024, H_ = 32, SK = 1024, DH = 128;
constexpr unsigned SLOT = 32768u;
constexpr float QSCALE = 0.12751743082f;
constexpr float THR = 8.0f;
constexpr unsigned WS_OFRAG = 0u;
constexpr unsigned WS_SLAB  = 8u << 20;
constexpr unsigned WS_FLAGO = 24u << 20;
constexpr unsigned WS_FLAGS = WS_FLAGO + 32768u;
constexpr unsigned WS_EPOCH = WS_FLAGS + 131072u;
constexpr unsigned SPIN_CAP = 1u << 20;

DI unsigned rfl(unsigned x) { return __builtin_amdgcn_readfirstlane(x); }
DI unsigned cvtpk(float lo, float hi) { f32x2 v = {lo, hi}; bf16x2 b = __builtin_convertvector(v, bf16x2); return __builtin_bit_cast(unsigned, b); }
DI bf16x8 pack8(const f32x16& x, int base) {
  u32x4 w = {cvtpk(x[base], x[base + 1]), cvtpk(x[base + 2], x[base + 3]), cvtpk(x[base + 4], x[base + 5]), cvtpk(x[base + 6], x[base + 7])};
  return __builtin_bit_cast(bf16x8, w);
}
DI float fadd_s(float a, float b) { float r; asm("v_add_f32_e32 %0, %1, %2" : "=v"(r) : "v"(a), "v"(b)); return r; }
DI float max3f(float a, float b, float c) { float r; asm("v_max3_f32 %0, %1, %2, %3" : "=v"(r) : "v"(a), "v"(b), "v"(c)); return r; }
template <int POL> DI void dma16(unsigned m0v, unsigned voff, __amdgpu_buffer_rsrc_t srd, unsigned soff) {
  if constexpr (POL == 1)      asm volatile("s_mov_b32 m0, %0\n\ts_nop 0\n\tbuffer_load_dwordx4 %1, %2, %3 offen nt lds" :: "s"(m0v), "v"(voff), "s"(srd), "s"(soff) : "memory");
  else if constexpr (POL == 2) asm volatile("s_mov_b32 m0, %0\n\ts_nop 0\n\tbuffer_load_dwordx4 %1, %2, %3 offen sc1 lds" :: "s"(m0v), "v"(voff), "s"(srd), "s"(soff) : "memory");
  else                         asm volatile("s_mov_b32 m0, %0\n\ts_nop 0\n\tbuffer_load_dwordx4 %1, %2, %3 offen lds" :: "s"(m0v), "v"(voff), "s"(srd), "s"(soff) : "memory");
}
DI void st16_sc1(u32x4 v, unsigned voff, __amdgpu_buffer_rsrc_t srd, unsigned soff) {
  asm volatile("buffer_store_dwordx4 %0, %1, %2, %3 offen sc1\n\ts_nop 1" :: "v"(v), "v"(voff), "s"(srd), "s"(soff) : "memory");
}
DI bf16x8 ld128(unsigned addr) { return *(LAS bf16x8*)addr; }
DI bf16x8 ldtr(unsigned a0, unsigned a1) {
  bf16x4 lo = __builtin_amdgcn_ds_read_tr16_b64_v4bf16((LAS bf16x4*)a0);
  bf16x4 hi = __builtin_amdgcn_ds_read_tr16_b64_v4bf16((LAS bf16x4*)a1);
  return __builtin_shufflevector(lo, hi, 0, 1, 2, 3, 4, 5, 6, 7);
}
DI unsigned fsw(unsigned row) { return (((row >> 1) & 1u) << 2) | ((row >> 2) & 3u); }
DI u64 ld_flag(const char* p) { return __hip_atomic_load((GAS u64*)p, __ATOMIC_RELAXED, __HIP_MEMORY_SCOPE_AGENT); }
DI void st_flag(char* p, u64 v) { __hip_atomic_store((GAS u64*)p, v, __ATOMIC_RELAXED, __HIP_MEMORY_SCOPE_AGENT); }

__global__ __launch_bounds__(512, 2)
void mha_fused(const char* __restrict__ x, const char* __restrict__ kc, const char* __restrict__ vc, const char* __restrict__ wq,
               const char* __restrict__ wo, char* __restrict__ out, char* ws) {
  extern __shared__ __attribute__((aligned(16))) char lds[];
  const unsigned tid = threadIdx.x, lane = tid & 63u, wid = rfl(tid >> 6), grp = wid >> 2, w4 = wid & 3u;
  const unsigned r = lane & 31u, hh = lane >> 5, blk = (lane >> 4) & 1u, q4 = (lane & 15u) >> 2, p4 = lane & 3u;
  const unsigned g = blockIdx.x & 7u, idx = blockIdx.x >> 3, bl = idx >> 3, hl = idx & 7u, kq = g >> 1;
  const unsigned b = 4u * (g & 1u) + bl, h = 8u * kq + hl, unit = b * 32u + h;
  STAMP(0);
  const unsigned ldsb = rfl((unsigned)(size_t)(LAS char*)lds);
  const __amdgpu_buffer_rsrc_t srdK = __builtin_amdgcn_make_buffer_rsrc((void*)(kc + (size_t)unit * (SK * DH * 2)), (short)0, SK * DH * 2, 0x00020000);
  const __amdgpu_buffer_rsrc_t srdV = __builtin_amdgcn_make_buffer_rsrc((void*)(vc + (size_t)unit * (SK * DH * 2)), (short)0, SK * DH * 2, 0x00020000);
  const unsigned rl = lane >> 3, ck0 = ((lane & 7u) ^ fsw(rl)) * 16u, ck1 = ck0 ^ 32u;
  const unsigned vkv0 = rl * 256u + ck0, vkv1 = rl * 256u + ck1;
  auto dma_kv = [&](unsigned t, unsigned slot) {
    const unsigned m = ldsb + slot * SLOT + w4 * 4096u, so = t * 16384u + w4 * 4096u;
    dma16<1>(m, vkv0, srdK, so);                  dma16<1>(m + 1024u, vkv0, srdK, so + 128u);
    dma16<1>(m + 2048u, vkv1, srdK, so + 2048u);  dma16<1>(m + 3072u, vkv1, srdK, so + 2176u);
    dma16<0>(m + 16384u, vkv0, srdV, so);         dma16<0>(m + 17408u, vkv0, srdV, so + 128u);
    dma16<0>(m + 18432u, vkv1, srdV, so + 2048u); dma16<0>(m + 19456u, vkv1, srdV, so + 2176u);
  };

  bf16x8 qf[8];
  u64 epoch;
  const char* epoch_p = ws + WS_EPOCH + b * 128u;
#define EPOCH_LOAD() asm volatile("s_load_dwordx2 %0, %1, 0x0" : "=s"(epoch) : "s"(epoch_p) : "memory")
#define EPOCH_WAIT() asm volatile("s_waitcnt lgkmcnt(0)" : "+s"(epoch) :: "memory")
  if (grp == 1) {
    const __amdgpu_buffer_rsrc_t srdX = __builtin_amdgcn_make_buffer_rsrc((void*)(x + (size_t)b * (SQ * DM * 2)), (short)0, SQ * DM * 2, 0x00020000);
    const __amdgpu_buffer_rsrc_t srdW = __builtin_amdgcn_make_buffer_rsrc((void*)(wq + h * 256u), (short)0, (int)(DM * H_ * DH * 2 - h * 256u), 0x00020000);
    const unsigned k0 = (2u * hl + 4u * bl) & 15u;
    const unsigned vx0 = rl * 2048u + ck0, vx1 = rl * 2048u + ck1, vw0 = rl * 8192u + ck0, vw1 = rl * 8192u + ck1;
    auto dma_q = [&](unsigned i) {
      const unsigned kt = (k0 + i) & 15u;
      const unsigned m = ldsb + (i & 3u) * SLOT + w4 * 4096u, sx = w4 * 32u * 2048u + kt * 128u, sw = (kt * 64u + w4 * 16u) * 8192u;
      dma16<0>(m, vx0, srdX, sx);                    dma16<0>(m + 1024u, vx1, srdX, sx + 16384u);
      dma16<0>(m + 2048u, vx0, srdX, sx + 32768u);   dma16<0>(m + 3072u, vx1, srdX, sx + 49152u);
      dma16<0>(m + 16384u, vw0, srdW, sw);           dma16<0>(m + 17408u, vw0, srdW, sw + 128u);
      dma16<0>(m + 18432u, vw1, srdW, sw + 65536u);  dma16<0>(m + 19456u, vw1, srdW, sw + 65664u);
    };
    dma_q(0); dma_q(1); dma_q(2);
    EPOCH_LOAD();
    WAITV(8); EPOCH_WAIT(); BARX();
    for (unsigned i = 0; i < 13; ++i) {
      dma_q(i + 3);
      if (i == 12) { dma_kv(0, 4); WAITV(16); }
      else WAITV(8);
      BARX();
    }
    WAITV(8); BARX();
    dma_kv(1, 0); BARX();
    dma_kv(2, 1);
  } else {
    f32x16 acc[4] = {};
    unsigned xb[4], wb[4];
#pragma unroll
    for (unsigned s = 0; s < 4; ++s) xb[s] = ldsb + 1024u * (4u * w4 + (r >> 3)) + 128u * (r & 7u) + 16u * ((2u * s + hh) ^ fsw(r));
#pragma unroll
    for (unsigned c = 0; c < 4; ++c) { const unsigned d1 = c >> 1, t = c & 1u;
      wb[c] = ldsb + 16384u + 2048u * hh + 128u * (4u * t + q4) + 16u * ((4u * d1 + 2u * blk + (p4 & 1u)) ^ ((((q4 >> 1) & 1u) << 2) | (2u * hh + t))) + 8u * (p4 >> 1); }
    EPOCH_LOAD(); EPOCH_WAIT();
    BARX();
    STAMP(1);
    bf16x8 xf[2], wf[2][4];
    unsigned xa[4], wa[4];
#pragma unroll
    for (int c = 0; c < 4; ++c) { xa[c] = xb[c]; wa[c] = wb[c]; }
    xf[0] = ld128(xa[0]);
#pragma unroll
    for (unsigned db = 0; db < 4; ++db) wf[0][db] = ldtr(wa[2 * (db & 1)] + 1024u * (db >> 1), wa[2 * (db & 1) + 1] + 1024u * (db >> 1));
    for (unsigned i = 0; i < 16; ++i) {
      const unsigned nso = ((i + 1u) & 3u) * SLOT;
      SB();
#pragma unroll
      for (unsigned s = 0; s < 4; ++s) {
        const unsigned cur = s & 1, nxt = cur ^ 1;
        if (s == 3) {
#pragma unroll
          for (int c = 0; c < 4; ++c) { xa[c] = xb[c] + nso; wa[c] = wb[c] + nso; OPAQUE(xa[c]); OPAQUE(wa[c]); }
        }
        const unsigned sn = (s + 1) & 3;
#pragma unroll
        for (unsigned db = 0; db < 4; ++db) {
          acc[db] = MFMA(wf[cur][db], xf[cur], acc[db]);
          if (db == 0) xf[nxt] = ld128(xa[sn]);
          const unsigned im = 4096u * sn + 1024u * (db >> 1);
          wf[nxt][db] = ldtr(wa[2 * (db & 1)] + im, wa[2 * (db & 1) + 1] + im);
          SB();
        }
      }
      if (i != 14) BARX();
    }
#pragma unroll
    for (int db = 0; db < 4; ++db) {
#pragma unroll
      for (int e = 0; e < 16; ++e) acc[db][e] *= QSCALE;
      qf[2 * db] = pack8(acc[db], 0); qf[2 * db + 1] = pack8(acc[db], 8);
    }
#pragma unroll
    for (int s = 0; s < 8; ++s) *(LAS bf16x8*)(ldsb + 3u * SLOT + w4 * 8192u + s * 1024u + lane * 16u) = qf[s];
  }
  STAMP(2);
  if (grp == 1) WAITV(8);
  BAR();
  if (grp == 1) {
#pragma unroll
    for (int s = 0; s < 8; ++s) qf[s] = ld128(ldsb + 3u * SLOT + w4 * 8192u + s * 1024u + lane * 16u);
  }
  BAR();
  STAMP(3);

  const __amdgpu_buffer_rsrc_t srdB = __builtin_amdgcn_make_buffer_rsrc((void*)(wo + (size_t)(kq * 1024u + grp * 512u) * 2048u + hl * 256u + w4 * 64u), (short)0, 512 * 2048 - 1984, 0x00020000);
  const unsigned vB = (lane >> 2) * 2048u + p4 * 16u;
  const unsigned prot = ((hl + bl) & 3u) * 8u;
  const unsigned bstage = ldsb + wid * 12288u;

  unsigned kb4[4], vb4[4];
#pragma unroll
  for (unsigned s = 0; s < 4; ++s) kb4[s] = ldsb + 2048u * (r >> 3) + 128u * (r & 7u) + 16u * ((2u * s + hh) ^ fsw(r));
#pragma unroll
  for (unsigned c = 0; c < 4; ++c) { const unsigned d1 = c >> 1, t = c & 1u;
    vb4[c] = ldsb + 16384u + 2048u * t + 128u * (4u * hh + q4) + 16u * ((4u * d1 + 2u * blk + (p4 & 1u)) ^ ((((q4 >> 1) & 1u) << 2) | (2u * t + hh))) + 8u * (p4 >> 1); }
  f32x16 o[4] = {};
  f32x16 negm = {};
  float mref = 0.f, lsum = 0.f, lsum2 = 0.f;
  if (grp == 0) __builtin_amdgcn_s_setprio(1);
  unsigned slot = grp ? 0u : 4u;
  unsigned lslot = grp ? 2u : 3u;
  for (unsigned k = 0; k < 8; ++k) {
    const unsigned so = slot * SLOT; slot = slot >= 3 ? slot - 3 : slot + 2;
    if (grp == 1 && k < 7u) dma_kv(2 * k + 3, lslot);
    if (grp == 1 && k == 7) {
#pragma unroll
      for (unsigned j = 0; j < 11; ++j) dma16<0>(bstage + j * 1024u, vB, srdB, ((prot + j) & 31u) * 32768u);
    }
    unsigned ka[4], va[4];
#pragma unroll
    for (int c = 0; c < 4; ++c) { ka[c] = kb4[c] + so; va[c] = vb4[c] + so; OPAQUE(ka[c]); OPAQUE(va[c]); }
    f32x16 s0, s1;
    bf16x8 kf[16], vf[16];
#define KRD(m) kf[m] = ld128(ka[(m) & 3] + 8192u * ((m) >> 3) + 1024u * (((m) >> 2) & 1))
#define VRD(m) vf[m] = ldtr(va[2 * ((m) & 1)] + 4096u * ((m) >> 2) + 1024u * (((m) >> 1) & 1), va[2 * ((m) & 1) + 1] + 4096u * ((m) >> 2) + 1024u * (((m) >> 1) & 1))
    KRD(0); SB(); KRD(1); SB(); KRD(2); SB(); KRD(3); SB(); KRD(4);
    SB();
    float mx;
#pragma unroll
    for (int m = 0; m < 16; ++m) {
      if (m == 0) asm volatile("v_mfma_f32_32x32x16_bf16 %0, %1, %2, %3" : "=&v"(s0) : "v"(kf[0]), "v"(qf[0]), "v"(negm));
      else if (m < 8) s0 = MFMA(kf[m], qf[m], s0);
      else if (m == 8) asm volatile("v_mfma_f32_32x32x16_bf16 %0, %1, %2, %3" : "=&v"(s1) : "v"(kf[8]), "v"(qf[0]), "v"(negm));
      else s1 = MFMA(kf[m], qf[m - 8], s1);
      if (m + 5 < 16) KRD(m + 5);
      if (m >= 12) VRD(m - 12);
      if (m == 10) mx = fmaxf(fmaxf(s0[0], s0[1]), s0[2]);
      if (m > 10) { mx = fmaxf(fmaxf(mx, s0[2 * m - 19]), s0[2 * m - 18]); }
      if (m == 15) { mx = fmaxf(fmaxf(mx, s0[13]), s0[14]); mx = fmaxf(mx, s0[15]); }
      SB();
    }
#pragma unroll
    for (int e = 0; e < 16; e += 2) mx = fmaxf(fmaxf(mx, s1[e]), s1[e + 1]);
    if (k == 0 || __any(mx > THR)) {
      auto pr = __builtin_amdgcn_permlane32_swap(__float_as_uint(mx), __float_as_uint(mx), false, false);
      float dl = fmaxf(__uint_as_float(pr[0]), __uint_as_float(pr[1]));
      if (k != 0) {
        dl = fmaxf(dl, 0.f);
        const float f = __builtin_amdgcn_exp2f(-dl);
        lsum *= f; lsum2 *= f;
#pragma unroll
        for (int db = 0; db < 4; ++db)
#pragma unroll
          for (int e = 0; e < 16; ++e) o[db][e] *= f;
      }
      mref += dl;
#pragma unroll
      for (int e = 0; e < 16; ++e) { s0[e] -= dl; s1[e] -= dl; negm[e] = -mref; }
    }
    bf16x8 pf[4];
#pragma unroll
    for (int e = 0; e < 8; ++e) s0[e] = __builtin_amdgcn_exp2f(s0[e]);
    pf[0] = pack8(s0, 0);
    SB();
#pragma unroll
    for (int m = 0; m < 16; ++m) {
      const int ks = m >> 2, db = m & 3;
      o[db] = MFMA(vf[m], pf[ks], o[db]);
      if (m + 4 < 16) VRD(m + 4);
      if (ks == 0) { s0[8 + 2 * db] = __builtin_amdgcn_exp2f(s0[8 + 2 * db]); s0[9 + 2 * db] = __builtin_amdgcn_exp2f(s0[9 + 2 * db]); lsum = fadd_s(lsum, s0[2 * db]); lsum2 = fadd_s(lsum2, s0[2 * db + 1]); if (db == 3) pf[1] = pack8(s0, 8); }
      if (ks == 1) { s1[2 * db] = __builtin_amdgcn_exp2f(s1[2 * db]); s1[1 + 2 * db] = __builtin_amdgcn_exp2f(s1[1 + 2 * db]); lsum = fadd_s(lsum, s0[8 + 2 * db]); lsum2 = fadd_s(lsum2, s0[9 + 2 * db]); if (db == 3) pf[2] = pack8(s1, 0); }
      if (ks == 2) { s1[8 + 2 * db] = __builtin_amdgcn_exp2f(s1[8 + 2 * db]); s1[9 + 2 * db] = __builtin_amdgcn_exp2f(s1[9 + 2 * db]); lsum = fadd_s(lsum, s1[2 * db]); lsum2 = fadd_s(lsum2, s1[2 * db + 1]); if (db == 3) pf[3] = pack8(s1, 8); }
      if (ks == 3) { lsum = fadd_s(lsum, s1[8 + 2 * db]); lsum2 = fadd_s(lsum2, s1[9 + 2 * db]); }
      SB();
    }
    if (k == 7) {
      if (grp == 0) {
#pragma unroll
        for (unsigned j = 0; j < 11; ++j) dma16<0>(bstage + j * 1024u, vB, srdB, ((prot + j) & 31u) * 32768u);
      }
      lsum += lsum2;
      auto pr = __builtin_amdgcn_permlane32_swap(__float_as_uint(lsum), __float_as_uint(lsum), false, false);
      lsum = __uint_as_float(pr[0]) + __uint_as_float(pr[1]);
      *(LAS f32x2*)(ldsb + (wid ^ 4u) * 12288u + 11264u + lane * 8u) = f32x2{mref, lsum};
    } else if (grp == 0) { if (k < 6) { dma_kv(2 * k + 4, lslot); WAITV(8); } else WAITV(0); }
    else WAITV(0);
    lslot = lslot >= 3 ? lslot - 3 : lslot + 2;
    BAR();
    STAMP(4 + k);
  }
  __builtin_amdgcn_s_setprio(0);

  const __amdgpu_buffer_rsrc_t srdWS = __builtin_amdgcn_make_buffer_rsrc((void*)ws, (short)0, (int)WS_FLAGO, 0x00020000);
  const unsigned l16 = lane * 16u;
  const u64 tag = epoch + 1ull;
  {
    const f32x2 ot = *(LAS f32x2*)(bstage + 11264u + lane * 8u);
    const float M = fmaxf(mref, ot[0]);
    const float wme = __builtin_amdgcn_exp2f(mref - M), wot = __builtin_amdgcn_exp2f(ot[0] - M);
    const float sc = wme / (wme * lsum + wot * ot[1]);
#pragma unroll
    for (int db = 0; db < 4; ++db)
#pragma unroll
      for (int e = 0; e < 16; ++e) o[db][e] *= sc;
    const unsigned sb = ldsb + 3u * SLOT + wid * 4096u + l16, rb = ldsb + 3u * SLOT + (wid ^ 4u) * 4096u + l16;
    f32x16 ka, kb_;
    if (grp == 0) {
#pragma unroll
      for (int j = 0; j < 4; ++j) { *(LAS f32x4*)(sb + SLOT + j * 1024u) = f32x4{o[2][4*j], o[2][4*j+1], o[2][4*j+2], o[2][4*j+3]};
                                    *(LAS f32x4*)(sb + j * 1024u) = f32x4{o[3][4*j], o[3][4*j+1], o[3][4*j+2], o[3][4*j+3]}; }
      ka = o[0]; kb_ = o[1];
    } else {
#pragma unroll
      for (int j = 0; j < 4; ++j) { *(LAS f32x4*)(sb + SLOT + j * 1024u) = f32x4{o[0][4*j], o[0][4*j+1], o[0][4*j+2], o[0][4*j+3]};
                                    *(LAS f32x4*)(sb + j * 1024u) = f32x4{o[1][4*j], o[1][4*j+1], o[1][4*j+2], o[1][4*j+3]}; }
      ka = o[2]; kb_ = o[3];
    }
    BAR();
#pragma unroll
    for (int j = 0; j < 4; ++j) {
      const f32x4 u = *(LAS f32x4*)(rb + SLOT + j * 1024u), v = *(LAS f32x4*)(rb + j * 1024u);
#pragma unroll
      for (int e = 0; e < 4; ++e) { ka[4*j+e] += u[e]; kb_[4*j+e] += v[e]; }
    }
    const bf16x8 f0 = pack8(ka, 0), f1 = pack8(ka, 8), f2 = pack8(kb_, 0), f3 = pack8(kb_, 8);
    const unsigned oo = WS_OFRAG + ((b * 4u + w4) * 256u + h * 8u + grp * 4u) * 1024u;
    st16_sc1(__builtin_bit_cast(u32x4, f0), l16, srdWS, oo);         st16_sc1(__builtin_bit_cast(u32x4, f1), l16, srdWS, oo + 1024u);
    st16_sc1(__builtin_bit_cast(u32x4, f2), l16, srdWS, oo + 2048u); st16_sc1(__builtin_bit_cast(u32x4, f3), l16, srdWS, oo + 3072u);
    *(LAS bf16x8*)(rb + SLOT) = f0; *(LAS bf16x8*)(rb + SLOT + 1024u) = f1; *(LAS bf16x8*)(rb + SLOT + 2048u) = f2; *(LAS bf16x8*)(rb + SLOT + 3072u) = f3;
  }
  STAMP(12);
  bf16x8 bw[32];
  const unsigned bt0 = bstage + 512u * hh + 64u * q4 + 32u * blk + 16u * (p4 & 1u) + 8u * (p4 >> 1);
  WAITV(4);
#pragma unroll
  for (unsigned j = 0; j < 11; ++j) bw[j] = ldtr(bt0 + j * 1024u, bt0 + j * 1024u + 256u);
  asm volatile("s_waitcnt lgkmcnt(0)" ::: "memory");
#pragma unroll
  for (unsigned j = 0; j < 4; ++j) dma16<0>(bstage + j * 1024u, vB, srdB, ((prot + 11u + j) & 31u) * 32768u);
  WAITV(4);
  if (lane == 0) st_flag(ws + WS_FLAGO + blockIdx.x * 128u + wid * 8u, tag);
#pragma unroll
  for (unsigned j = 4; j < 11; ++j) dma16<0>(bstage + j * 1024u, vB, srdB, ((prot + 11u + j) & 31u) * 32768u);
  STAMP(13);
  BARX();
  WAITV(0);
#pragma unroll
  for (unsigned j = 0; j < 11; ++j) bw[11 + j] = ldtr(bt0 + j * 1024u, bt0 + j * 1024u + 256u);
  asm volatile("s_waitcnt lgkmcnt(0)" ::: "memory");
#pragma unroll
  for (unsigned j = 0; j < 10; ++j) dma16<0>(bstage + j * 1024u, vB, srdB, ((prot + 22u + j) & 31u) * 32768u);
  WAITV(0);
#pragma unroll
  for (unsigned j = 0; j < 10; ++j) bw[22 + j] = ldtr(bt0 + j * 1024u, bt0 + j * 1024u + 256u);
  STAMP(14);
  if (wid == 0) {
    const char* fp = ws + WS_FLAGO + (((blockIdx.x & ~56u) | ((lane >> 3) << 3)) * 128u) + (lane & 7u) * 8u;
    for (unsigned sp = 0; sp < SPIN_CAP; ++sp) {
      if (__all(ld_flag(fp) == tag)) break;
      __builtin_amdgcn_s_sleep(1);
    }
  }
  BAR();
  STAMP(15);
  const unsigned ah = (wid + hl) & 7u;
  auto dma_a = [&](unsigned i) {
    const unsigned rbk = (kq + 1u + i) & 3u;
    const unsigned m = ldsb + (i & 1u) * 65536u + ah * 8192u, so = WS_OFRAG + ((b * 4u + rbk) * 256u + (kq * 8u + ah) * 8u) * 1024u;
#pragma unroll
    for (unsigned j = 0; j < 8; ++j) dma16<2>(m + j * 1024u, l16, srdWS, so + j * 1024u);
  };
  dma_a(0); dma_a(1);
  WAITV(8); BARX();
  STAMP(16);
  const unsigned red = ldsb + 4u * SLOT;
  const unsigned abase = ldsb + grp * 32768u + l16;
  f32x16 prev;
#pragma unroll
  for (unsigned i = 0; i < 4; ++i) {
    unsigned aa[4];
#pragma unroll
    for (unsigned c = 0; c < 4; ++c) { aa[c] = abase + (i & 1u) * 65536u + ((prot + 8u * c) & 31u) * 1024u; OPAQUE(aa[c]); }
    bf16x8 af[32];
#define ARD(m) af[m] = ld128(aa[(m) >> 3] + 1024u * ((m) & 7))
    ARD(0); SB(); ARD(1); SB(); ARD(2); SB(); ARD(3); SB();
    f32x4 u0, u1;
    if (i > 0) { u0 = *(LAS f32x4*)(red + ((i - 1u) & 1u) * 16384u + (wid ^ 4u) * 2048u + l16); u1 = *(LAS f32x4*)(red + ((i - 1u) & 1u) * 16384u + (wid ^ 4u) * 2048u + 1024u + l16); }
    SB();
    f32x16 acc = {};
#pragma unroll
    for (int m = 0; m < 32; ++m) {
      acc = MFMA(bw[m], af[m], acc);
      if (m + 4 < 32) ARD(m + 4);
      if (i > 0 && m == 2) {
        const unsigned dst = (blockIdx.x & ~6u) | (((kq + i) & 3u) << 1);
        const unsigned so = WS_SLAB + (dst * 4u + kq) * 16384u + wid * 2048u;
        f32x4 a0, a1;
        if (grp == 0) { a0 = f32x4{prev[0], prev[1], prev[2], prev[3]}; a1 = f32x4{prev[4], prev[5], prev[6], prev[7]}; }
        else          { a0 = f32x4{prev[8], prev[9], prev[10], prev[11]}; a1 = f32x4{prev[12], prev[13], prev[14], prev[15]}; }
        a0 += u0; a1 += u1;
        st16_sc1(__builtin_bit_cast(u32x4, a0), l16, srdWS, so); st16_sc1(__builtin_bit_cast(u32x4, a1), l16, srdWS, so + 1024u);
        if (i < 3) dma_a(i + 1);
      }
      if (i > 0 && m == 20) {
        if (i < 3) WAITV(8); else WAITV(0);
        const unsigned dst = (blockIdx.x & ~6u) | (((kq + i) & 3u) << 1);
        if (lane == 0) st_flag(ws + WS_FLAGS + (dst * 4u + kq) * 128u + wid * 8u, tag);
      }
      SB();
    }
    {
      const unsigned rw = red + (i & 1u) * 16384u + wid * 2048u + l16;
      if (grp == 0) { *(LAS f32x4*)(rw) = f32x4{acc[8], acc[9], acc[10], acc[11]}; *(LAS f32x4*)(rw + 1024u) = f32x4{acc[12], acc[13], acc[14], acc[15]}; }
      else          { *(LAS f32x4*)(rw) = f32x4{acc[0], acc[1], acc[2], acc[3]};   *(LAS f32x4*)(rw + 1024u) = f32x4{acc[4], acc[5], acc[6], acc[7]}; }
    }
    prev = acc;
    if (i == 0) WAITV(0); else if (i < 3) WAITV(1);
    BAR();
    STAMP(17 + i);
  }
  {
    f32x4 a0, a1;
    if (grp == 0) { a0 = f32x4{prev[0], prev[1], prev[2], prev[3]}; a1 = f32x4{prev[4], prev[5], prev[6], prev[7]}; }
    else          { a0 = f32x4{prev[8], prev[9], prev[10], prev[11]}; a1 = f32x4{prev[12], prev[13], prev[14], prev[15]}; }
    a0 += *(LAS f32x4*)(red + 16384u + (wid ^ 4u) * 2048u + l16); a1 += *(LAS f32x4*)(red + 16384u + (wid ^ 4u) * 2048u + 1024u + l16);
    const unsigned src = (kq + 1u + (lane & 3u) % 3u) & 3u;
    const char* fp = ws + WS_FLAGS + (blockIdx.x * 4u + src) * 128u + wid * 8u;
    for (unsigned sp = 0; sp < SPIN_CAP; ++sp) {
      if (__all(ld_flag(fp) == tag)) break;
      __builtin_amdgcn_s_sleep(1);
    }
    asm volatile("" ::: "memory");
    STAMP(21);
#pragma unroll
    for (unsigned j = 1; j < 4; ++j) {
      const unsigned so = WS_SLAB + (blockIdx.x * 4u + ((kq + j) & 3u)) * 16384u + wid * 2048u;
      a0 += __builtin_bit_cast(f32x4, __builtin_amdgcn_raw_buffer_load_b128(srdWS, l16, so, 16));
      a1 += __builtin_bit_cast(f32x4, __builtin_amdgcn_raw_buffer_load_b128(srdWS, l16, so + 1024u, 16));
    }
    const u32x4 ov = {cvtpk(a0[0], a0[1]), cvtpk(a0[2], a0[3]), cvtpk(a1[0], a1[1]), cvtpk(a1[2], a1[3])};
    *(u32x4*)(out + (size_t)(b * 128u + kq * 32u + r) * 2048u + hl * 256u + w4 * 64u + grp * 32u + hh * 16u) = ov;
    if (h == 0 && tid == 0) st_flag(ws + WS_EPOCH + b * 128u, tag);
  }
  STAMP(22);
}

extern "C" void kernel_launch(void* const* d_in, const int* in_sizes, int n_in, void* d_out, int out_size, void* d_ws, size_t ws_size, hipStream_t stream) {
  (void)in_sizes; (void)n_in; (void)out_size; (void)ws_size;
  (void)hipFuncSetAttribute((const void*)mha_fused, hipFuncAttributeMaxDynamicSharedMemorySize, 163840);
  mha_fused<<<256, 512, 163840, stream>>>((const char*)d_in[0], (const char*)d_in[1], (const char*)d_in[2], (const char*)d_in[3], (const char*)d_in[4], (char*)d_out, (char*)d_ws);
}
```

```cpp
#include <hip/hip_runtime.h>
#include <hip/hip_bf16.h>

typedef __bf16   bf16x8 __attribute__((ext_vector_type(8)));
typedef __bf16   bf16x4 __attribute__((ext_vector_type(4)));
typedef __bf16   bf16x2 __attribute__((ext_vector_type(2)));
typedef float    f32x4  __attribute__((ext_vector_type(4)));
typedef float    f32x2  __attribute__((ext_vector_type(2)));
typedef unsigned u32x4  __attribute__((ext_vector_type(4)));
typedef unsigned u32x2  __attribute__((ext_vector_type(2)));
typedef unsigned long long u64;
#define LAS __attribute__((address_space(3)))
#define GAS __attribute__((address_space(1)))
#define DI __device__ __forceinline__
#define MFMA(a, b, c) __builtin_amdgcn_mfma_f32_16x16x32_bf16((a), (b), (c), 0, 0, 0)
#define WAITV(n) asm volatile("s_waitcnt vmcnt(" #n ")" ::: "memory")
#define WAITL0() asm volatile("s_waitcnt lgkmcnt(0)" ::: "memory")
#define SB() __builtin_amdgcn_sched_barrier(0)
#define OPAQUE(x) asm volatile("" : "+v"(x))
#define BAR() do { asm volatile("s_waitcnt lgkmcnt(0)" ::: "memory"); __builtin_amdgcn_s_barrier(); asm volatile("" ::: "memory"); } while (0)
#define BARX() do { asm volatile("" ::: "memory"); __builtin_amdgcn_s_barrier(); asm volatile("" ::: "memory"); } while (0)
#define STAMP(i)
#define KV_EARLY 7
#define PACE 0

constexpr int SQ = 128, DM = 1024, H_ = 32, SK = 1024, DH = 128;
constexpr unsigned SLOT = 32768u;
constexpr float QSCALE = 0.12751743082f;
constexpr float THR = 8.0f;
constexpr unsigned WS_OFRAG = 0u;
constexpr unsigned WS_SLAB  = 8u << 20;
constexpr unsigned WS_FLAGO = 24u << 20;
constexpr unsigned WS_FLAGS = WS_FLAGO + 32768u;
constexpr unsigned WS_EPOCH = WS_FLAGS + 131072u;
constexpr unsigned SPIN_CAP = 1u << 20;

DI unsigned rfl(unsigned x) { return __builtin_amdgcn_readfirstlane(x); }
DI unsigned cvtpk(float lo, float hi) { f32x2 v = {lo, hi}; bf16x2 b = __builtin_convertvector(v, bf16x2); return __builtin_bit_cast(unsigned, b); }
DI bf16x8 pack8(const f32x4& x, const f32x4& y) {
  u32x4 w = {cvtpk(x[0], x[1]), cvtpk(x[2], x[3]), cvtpk(y[0], y[1]), cvtpk(y[2], y[3])};
  return __builtin_bit_cast(bf16x8, w);
}
DI float fadd_s(float a, float b) { float r; asm("v_add_f32_e32 %0, %1, %2" : "=v"(r) : "v"(a), "v"(b)); return r; }
DI float ex2(float x) { return __builtin_amdgcn_exp2f(x); }
DI float red4_max(float x) {
  auto a = __builtin_amdgcn_permlane32_swap(__float_as_uint(x), __float_as_uint(x), false, false);
  x = fmaxf(__uint_as_float(a[0]), __uint_as_float(a[1]));
  auto b = __builtin_amdgcn_permlane16_swap(__float_as_uint(x), __float_as_uint(x), false, false);
  return fmaxf(__uint_as_float(b[0]), __uint_as_float(b[1]));
}
DI float red4_sum(float x) {
  auto a = __builtin_amdgcn_permlane32_swap(__float_as_uint(x), __float_as_uint(x), false, false);
  x = __uint_as_float(a[0]) + __uint_as_float(a[1]);
  auto b = __builtin_amdgcn_permlane16_swap(__float_as_uint(x), __float_as_uint(x), false, false);
  return __uint_as_float(b[0]) + __uint_as_float(b[1]);
}
template <int POL> DI void dma16(unsigned m0v, unsigned voff, __amdgpu_buffer_rsrc_t srd, unsigned soff) {
  if constexpr (POL == 1)      asm volatile("s_mov_b32 m0, %0\n\ts_nop 0\n\tbuffer_load_dwordx4 %1, %2, %3 offen nt lds" :: "s"(m0v), "v"(voff), "s"(srd), "s"(soff) : "memory");
  else if constexpr (POL == 2) asm volatile("s_mov_b32 m0, %0\n\ts_nop 0\n\tbuffer_load_dwordx4 %1, %2, %3 offen sc1 lds" :: "s"(m0v), "v"(voff), "s"(srd), "s"(soff) : "memory");
  else                         asm volatile("s_mov_b32 m0, %0\n\ts_nop 0\n\tbuffer_load_dwordx4 %1, %2, %3 offen lds" :: "s"(m0v), "v"(voff), "s"(srd), "s"(soff) : "memory");
}
DI void st16_sc1(u32x4 v, unsigned voff, __amdgpu_buffer_rsrc_t srd, unsigned soff) {
  asm volatile("buffer_store_dwordx4 %0, %1, %2, %3 offen sc1\n\ts_nop 1" :: "v"(v), "v"(voff), "s"(srd), "s"(soff) : "memory");
}
DI bf16x8 ld128(unsigned addr) { return *(LAS bf16x8*)addr; }
DI bf16x8 ldtr(unsigned a0, unsigned a1) {
  bf16x4 lo = __builtin_amdgcn_ds_read_tr16_b64_v4bf16((LAS bf16x4*)a0);
  bf16x4 hi = __builtin_amdgcn_ds_read_tr16_b64_v4bf16((LAS bf16x4*)a1);
  return __builtin_shufflevector(lo, hi, 0, 1, 2, 3, 4, 5, 6, 7);
}
DI unsigned fr(unsigned row) { return ((row >> 1) & 7u) ^ (((row >> 2) ^ (row >> 3)) & 1u); }
DI u64 ld_flag(const char* p) { return __hip_atomic_load((GAS u64*)p, __ATOMIC_RELAXED, __HIP_MEMORY_SCOPE_AGENT); }
DI void st_flag(char* p, u64 v) { __hip_atomic_store((GAS u64*)p, v, __ATOMIC_RELAXED, __HIP_MEMORY_SCOPE_AGENT); }
DI unsigned kaddr(unsigned m) { return m == 2u ? 16384u : 131072u + 16384u * m; }
DI unsigned vaddr(unsigned m) { return m == 0u ? 0u : 16384u + 16384u * m; }

__global__ __launch_bounds__(512, 2)
void mha_fused(const char* __restrict__ x, const char* __restrict__ kc, const char* __restrict__ vc, const char* __restrict__ wq,
               const char* __restrict__ wo, char* __restrict__ out, char* ws) {
  extern __shared__ __attribute__((aligned(16))) char lds[];
  const unsigned tid = threadIdx.x, lane = tid & 63u, wid = rfl(tid >> 6), grp = wid >> 2, w4 = wid & 3u;
  const unsigned c = lane & 15u, g = lane >> 4, q4 = c >> 2, p4 = lane & 3u, l16 = lane * 16u;
  const unsigned xg = blockIdx.x & 7u, idx = blockIdx.x >> 3, bl = idx >> 3, hl = idx & 7u, kq = xg >> 1;
  const unsigned b = 4u * (xg & 1u) + bl, h = 8u * kq + hl, unit = b * 32u + h;
  STAMP(0);
  const unsigned ldsb = rfl((unsigned)(size_t)(LAS char*)lds);
  const __amdgpu_buffer_rsrc_t srdK = __builtin_amdgcn_make_buffer_rsrc((void*)(kc + (size_t)unit * (SK * DH * 2)), (short)0, SK * DH * 2, 0x00020000);
  const __amdgpu_buffer_rsrc_t srdV = __builtin_amdgcn_make_buffer_rsrc((void*)(vc + (size_t)unit * (SK * DH * 2)), (short)0, SK * DH * 2, 0x00020000);
  const unsigned rl = lane >> 3, ckr = ((lane & 7u) ^ fr(rl)) * 16u;
  const unsigned vk0 = rl * 256u + ckr, vk1 = rl * 256u + (ckr ^ 80u), vv = rl * 256u + (((lane & 7u) ^ (rl & 6u)) * 16u);
  auto dma_k = [&](unsigned t, unsigned la) {
    const unsigned m = ldsb + la + w4 * 4096u, so = t * 16384u + w4 * 4096u;
    dma16<1>(m, vk0, srdK, so);                  dma16<1>(m + 1024u, vk0, srdK, so + 128u);
    dma16<1>(m + 2048u, vk1, srdK, so + 2048u);  dma16<1>(m + 3072u, vk1, srdK, so + 2176u);
  };
  auto dma_v = [&](unsigned t, unsigned la) {
    const unsigned m = ldsb + la + w4 * 4096u, so = t * 16384u + w4 * 4096u;
    dma16<0>(m, vv, srdV, so);                  dma16<0>(m + 1024u, vv, srdV, so + 128u);
    dma16<0>(m + 2048u, vv, srdV, so + 2048u);  dma16<0>(m + 3072u, vv, srdV, so + 2176u);
  };

  u64 epoch;
  const char* epoch_p = ws + WS_EPOCH + b * 128u;
#define EPOCH_LOAD() asm volatile("s_load_dwordx2 %0, %1, 0x0" : "=s"(epoch) : "s"(epoch_p) : "memory")
#define EPOCH_WAIT() asm volatile("s_waitcnt lgkmcnt(0)" : "+s"(epoch) :: "memory")
  if (grp == 1) {
    const __amdgpu_buffer_rsrc_t srdX = __builtin_amdgcn_make_buffer_rsrc((void*)(x + (size_t)b * (SQ * DM * 2)), (short)0, SQ * DM * 2, 0x00020000);
    const __amdgpu_buffer_rsrc_t srdW = __builtin_amdgcn_make_buffer_rsrc((void*)(wq + h * 256u), (short)0, (int)(DM * H_ * DH * 2 - h * 256u), 0x00020000);
    const unsigned k0 = (2u * hl + 4u * bl) & 15u;
    const unsigned ckw = ((lane & 7u) ^ (rl & 2u)) * 16u;
    const unsigned vx0 = rl * 2048u + ckr, vx1 = rl * 2048u + (ckr ^ 80u), vw0 = rl * 8192u + ckw, vw1 = rl * 8192u + (ckw ^ 64u);
    auto dma_q = [&](unsigned i) {
      const unsigned kt = (k0 + i) & 15u;
      const unsigned m = ldsb + (i & 3u) * SLOT + w4 * 4096u, sx = w4 * 32u * 2048u + kt * 128u, sw = (kt * 64u + w4 * 16u) * 8192u;
      dma16<0>(m, vx0, srdX, sx);                    dma16<0>(m + 1024u, vx1, srdX, sx + 16384u);
      dma16<0>(m + 2048u, vx0, srdX, sx + 32768u);   dma16<0>(m + 3072u, vx1, srdX, sx + 49152u);
      dma16<0>(m + 16384u, vw0, srdW, sw);           dma16<0>(m + 17408u, vw0, srdW, sw + 128u);
      dma16<0>(m + 18432u, vw1, srdW, sw + 65536u);  dma16<0>(m + 19456u, vw1, srdW, sw + 65664u);
    };
    dma_q(0); dma_q(1); dma_q(2);
    EPOCH_LOAD();
    WAITV(8); EPOCH_WAIT(); BARX();
    for (unsigned i = 0; i < 13; ++i) {
      dma_q(i + 3);
      if (i == KV_EARLY) { dma_k(0, kaddr(0)); dma_k(1, kaddr(1)); }
      if (i == KV_EARLY || i == KV_EARLY + 1) WAITV(16); else WAITV(8);
      BARX();
    }
    dma_v(0, vaddr(0)); dma_k(2, kaddr(2));
    WAITV(8); BARX();
    dma_v(1, vaddr(1)); BARX();
  } else {
    const unsigned dh = w4 & 1u, qh = w4 >> 1;
    f32x4 acc[4][4] = {};
    unsigned xb[2], wb[4];
#pragma unroll
    for (unsigned s = 0; s < 2; ++s) xb[s] = ldsb + 8192u * qh + 1024u * (c >> 3) + 128u * (c & 7u) + 16u * ((4u * s + g) ^ fr(c));
#pragma unroll
    for (unsigned i = 0; i < 4; ++i) wb[i] = ldsb + 16384u + 2048u * g + 1024u * dh + 128u * q4 + 16u * ((2u * i + (p4 >> 1)) ^ ((q4 & 2u) | ((g & 1u) << 2))) + 8u * (p4 & 1u);
    EPOCH_LOAD(); EPOCH_WAIT();
    BARX();
    STAMP(1);
    bf16x8 xf[2][4], wf[2][4];
    unsigned xa[2], wa[4];
#pragma unroll
    for (int i = 0; i < 4; ++i) { wa[i] = wb[i]; wf[0][i] = ldtr(wa[i], wa[i] + 512u); xf[0][i] = ld128(xb[0] + 2048u * i); }
    xa[0] = xb[0]; xa[1] = xb[1];
    for (unsigned it = 0; it < 16; ++it) {
      const unsigned nso = ((it + 1u) & 3u) * SLOT;
      SB();
#pragma unroll
      for (unsigned s = 0; s < 2; ++s) {
        if (s == 1) {
#pragma unroll
          for (int i = 0; i < 4; ++i) { wa[i] = wb[i] + nso; OPAQUE(wa[i]); }
          xa[0] = xb[0] + nso; OPAQUE(xa[0]);
        }
#pragma unroll
        for (unsigned m = 0; m < 16; ++m) {
          const unsigned i = m >> 2, j = m & 3;
          acc[i][j] = MFMA(wf[s][i], xf[s][j], acc[i][j]);
          if (m < 4) xf[s ^ 1][m] = ld128(xa[s ^ 1] + 2048u * m);
          if (m >= 4 && m < 12 && (m & 1) == 0) { const unsigned n = (m - 4) >> 1; wf[s ^ 1][n] = ldtr(wa[n] + 8192u * (s ^ 1), wa[n] + 8192u * (s ^ 1) + 512u); }
          SB();
        }
        if (s == 0) { xa[1] = xb[1] + nso; OPAQUE(xa[1]); }
      }
      if (it != 14) BARX();
      STAMP(32 + it);
    }
#pragma unroll
    for (unsigned i = 0; i < 4; ++i)
#pragma unroll
      for (unsigned j = 0; j < 4; ++j) {
        const u32x2 w = {cvtpk(acc[i][j][0] * QSCALE, acc[i][j][1] * QSCALE), cvtpk(acc[i][j][2] * QSCALE, acc[i][j][3] * QSCALE)};
        *(LAS u32x2*)(ldsb + 3u * SLOT + ((4u * qh + j) * 4u + 2u * dh + (i >> 1)) * 1024u + (16u * (2u * (i & 1u) + (g >> 1)) + c) * 16u + 8u * (g & 1u)) = w;
      }
  }
  STAMP(2);
  BAR();
  bf16x8 qf[2][4];
#pragma unroll
  for (int s = 0; s < 8; ++s) qf[s >> 2][s & 3] = ld128(ldsb + 3u * SLOT + w4 * 8192u + s * 1024u + l16);
  STAMP(3);

  const __amdgpu_buffer_rsrc_t srdB = __builtin_amdgcn_make_buffer_rsrc((void*)(wo + (size_t)(kq * 1024u + grp * 512u) * 2048u + hl * 256u + w4 * 64u), (short)0, 512 * 2048 - 1984, 0x00020000);
  const unsigned vB = (lane >> 2) * 2048u + ((p4 ^ ((g & 1u) << 1)) * 16u);
  const unsigned prot = ((hl + bl) & 3u) * 8u;
  const unsigned bstage = ldsb + 65536u + wid * 8192u;
  auto dma_b4 = [&](unsigned p0) {
#pragma unroll
    for (unsigned j = 0; j < 4; ++j) dma16<0>(bstage + ((p0 + j) & 7u) * 1024u, vB, srdB, ((prot + p0 + j) & 31u) * 32768u);
  };

  unsigned kb2[2], vb4[4];
#pragma unroll
  for (unsigned s = 0; s < 2; ++s) kb2[s] = ldsb + 2048u * (c >> 3) + 128u * (c & 7u) + 16u * ((4u * s + g) ^ fr(c));
#pragma unroll
  for (unsigned i = 0; i < 4; ++i) vb4[i] = ldsb + 2048u * (g >> 1) + 128u * (4u * (g & 1u) + q4) + 16u * ((2u * i + (p4 >> 1)) ^ ((q4 & 2u) | ((g & 1u) << 2))) + 8u * (p4 & 1u);
  f32x4 o[8][2] = {};
  f32x4 negm[2] = {};
  float mref[2] = {0.f, 0.f}, ls[2][2] = {{0.f, 0.f}, {0.f, 0.f}};
  if (grp == 1) { WAITV(8); BAR(); }
  unsigned jm = grp;
  for (unsigned it = 0; it < 8; ++it) {
    const unsigned j = 2u * it + grp;
    const unsigned jm1 = jm == 2u ? 0u : jm + 1u;
    f32x4 s[4][2];
    {
      unsigned ka[2];
      const unsigned so = kaddr(jm);
#pragma unroll
      for (int i = 0; i < 2; ++i) { ka[i] = kb2[i] + so; OPAQUE(ka[i]); }
      bf16x8 kf[16];
#define KRD(m) kf[m] = ld128(ka[(m) & 1] + 4096u * ((m) >> 2) + 1024u * (((m) >> 1) & 1))
      KRD(0); SB(); KRD(1); SB(); KRD(2); SB(); KRD(3); SB(); KRD(4);
      SB();
      float mx[2];
#pragma unroll
      for (int m = 0; m < 16; ++m) {
        const int kt = m >> 2, ds = m & 3;
#pragma unroll
        for (int qt = 0; qt < 2; ++qt) {
          s[kt][qt] = MFMA(kf[m], qf[qt][ds], ds == 0 ? negm[qt] : s[kt][qt]);
          if (qt == 0 && m + 5 < 16) KRD(m + 5);
          if (kt > 0 && ds >= 2) {
            const int e = 2 * (ds - 2);
            if (kt == 1 && ds == 2) mx[qt] = fmaxf(s[0][qt][0], s[0][qt][1]);
            else mx[qt] = fmaxf(fmaxf(mx[qt], s[kt - 1][qt][e]), s[kt - 1][qt][e + 1]);
          }
          SB();
        }
        if (PACE && ds == 3 && j < 14) __builtin_amdgcn_s_sleep(PACE);
      }
#pragma unroll
      for (int qt = 0; qt < 2; ++qt) { mx[qt] = fmaxf(fmaxf(mx[qt], s[3][qt][0]), s[3][qt][1]); mx[qt] = fmaxf(fmaxf(mx[qt], s[3][qt][2]), s[3][qt][3]); }
      if (it == 0 || __any(fmaxf(mx[0], mx[1]) > THR)) {
#pragma unroll
        for (int qt = 0; qt < 2; ++qt) {
          float dl = red4_max(mx[qt]);
          if (it != 0) {
            dl = fmaxf(dl, 0.f);
            const float f = ex2(-dl);
            ls[qt][0] *= f; ls[qt][1] *= f;
#pragma unroll
            for (int dt = 0; dt < 8; ++dt) o[dt][qt] *= f;
          }
          mref[qt] += dl;
#pragma unroll
          for (int kt = 0; kt < 4; ++kt) s[kt][qt] -= dl;
          negm[qt] = f32x4{-mref[qt], -mref[qt], -mref[qt], -mref[qt]};
        }
      }
    }
    bf16x8 pf[2][2];
#pragma unroll
    for (int qt = 0; qt < 2; ++qt)
#pragma unroll
      for (int e = 0; e < 4; ++e) { s[0][qt][e] = ex2(s[0][qt][e]); s[1][qt][e] = ex2(s[1][qt][e]); }
    SB();
    pf[0][0] = pack8(s[0][0], s[1][0]); pf[0][1] = pack8(s[0][1], s[1][1]);
    OPAQUE(pf[0][0]); OPAQUE(pf[0][1]);
    SB();
#pragma unroll
    for (int e = 0; e < 4; ++e)
#pragma unroll
      for (int qt = 0; qt < 2; ++qt) { ls[qt][0] = fadd_s(ls[qt][0], s[0][qt][e]); ls[qt][1] = fadd_s(ls[qt][1], s[1][qt][e]); }
    SB();
    if (it == 3) STAMP(48); if (it == 7) STAMP(52);
    if (j - 1u < 14u) { dma_v(j + 1u, vaddr(jm1)); WAITV(4); }
    else if (j == 15u) { dma_b4(0); dma_b4(4); }
    if (it == 3) STAMP(49); if (it == 7) STAMP(53);
    BARX();
    STAMP(4 + 2 * it);
    {
      if (j <= 12u) dma_k(j + 3u, kaddr(jm));
      else if (j == 14u) { dma_b4(0); dma_b4(4); }
      if (it == 3) STAMP(50); if (it == 7) STAMP(54);
      unsigned va[4];
      const unsigned so = vaddr(jm);
#pragma unroll
      for (int i = 0; i < 4; ++i) { va[i] = vb4[i] + so; OPAQUE(va[i]); }
      bf16x8 vf[16];
#define VRD(m) vf[m] = ldtr(va[(m) & 3] + 8192u * ((m) >> 3) + 1024u * (((m) >> 2) & 1), va[(m) & 3] + 8192u * ((m) >> 3) + 1024u * (((m) >> 2) & 1) + 4096u)
      VRD(0); SB(); VRD(1); SB(); VRD(2); SB(); VRD(3);
      SB();
#pragma unroll
      for (int m = 0; m < 16; ++m) {
        const int ks = m >> 3, dt = m & 7;
#pragma unroll
        for (int qt = 0; qt < 2; ++qt) {
          o[dt][qt] = MFMA(vf[m], pf[ks][qt], o[dt][qt]);
          if (qt == 0 && m + 4 < 16) VRD(m + 4);
          if (ks == 0) { const int kt = 2 + (dt >> 2), e = dt & 3; s[kt][qt][e] = ex2(s[kt][qt][e]); if (dt == 7) pf[1][qt] = pack8(s[2][qt], s[3][qt]); }
          else { const int kt = 2 + (dt >> 2), e = dt & 3; ls[qt][dt & 1] = fadd_s(ls[qt][dt & 1], s[kt][qt][e]); }
          SB();
        }
        if (PACE && (dt & 3) == 3 && j < 14) __builtin_amdgcn_s_sleep(PACE);
      }
      if (it == 3) STAMP(51); if (it == 7) STAMP(55);
      if (j <= 12u) WAITV(4); else if (j == 13u) WAITV(0); else if (j == 14u) WAITV(8);
    }
    jm = jm == 0u ? 2u : jm - 1u;
    BARX();
    STAMP(5 + 2 * it);
  }

  const __amdgpu_buffer_rsrc_t srdWS = __builtin_amdgcn_make_buffer_rsrc((void*)ws, (short)0, (int)WS_FLAGO, 0x00020000);
  const u64 tag = epoch + 1ull;
  const unsigned xo0 = ldsb + 131072u + w4 * 8192u + l16, xo1 = ldsb + 32768u + w4 * 8192u + l16, xml = ldsb + 16384u + w4 * 1024u + l16;
  const float lt0 = red4_sum(ls[0][0] + ls[0][1]), lt1 = red4_sum(ls[1][0] + ls[1][1]);
  if (grp == 0) {
#pragma unroll
    for (int dt = 0; dt < 4; ++dt)
#pragma unroll
      for (int qt = 0; qt < 2; ++qt) { *(LAS f32x4*)(xo0 + (2 * dt + qt) * 1024u) = o[dt][qt]; *(LAS f32x4*)(xo1 + (2 * dt + qt) * 1024u) = o[4 + dt][qt]; }
    *(LAS f32x4*)xml = f32x4{mref[0], lt0, mref[1], lt1};
    BAR();
  } else {
    const f32x4 ot = *(LAS f32x4*)xml;
    float wa_[2], wb_[2];
    {
      const float M0 = fmaxf(mref[0], ot[0]), M1 = fmaxf(mref[1], ot[2]);
      const float a0 = ex2(mref[0] - M0), b0 = ex2(ot[0] - M0), a1 = ex2(mref[1] - M1), b1 = ex2(ot[2] - M1);
      const float r0 = __builtin_amdgcn_rcpf(a0 * lt0 + b0 * ot[1]), r1 = __builtin_amdgcn_rcpf(a1 * lt1 + b1 * ot[3]);
      wa_[0] = a0 * r0; wb_[0] = b0 * r0; wa_[1] = a1 * r1; wb_[1] = b1 * r1;
    }
#pragma unroll
    for (int s2 = 0; s2 < 4; ++s2)
#pragma unroll
      for (int qt = 0; qt < 2; ++qt) {
        const unsigned xo = s2 < 2 ? xo0 : xo1;
        const f32x4 u = *(LAS f32x4*)(xo + (4 * (s2 & 1) + qt) * 1024u), v = *(LAS f32x4*)(xo + (4 * (s2 & 1) + 2 + qt) * 1024u);
        const bf16x8 f = pack8(o[2 * s2][qt] * wa_[qt] + u * wb_[qt], o[2 * s2 + 1][qt] * wa_[qt] + v * wb_[qt]);
        st16_sc1(__builtin_bit_cast(u32x4, f), l16, srdWS, WS_OFRAG + ((b * 8u + 2u * w4 + qt) * 128u + h * 4u + s2) * 1024u);
      }
  }
  STAMP(20);
  bf16x8 bw[16][2];
  const unsigned bt0 = bstage + 64u * (4u * g + q4) + 32u * (g & 1u) + 8u * p4, bt1 = bt0 ^ 32u;
#pragma unroll
  for (unsigned rd = 0; rd < 8; ++rd) {
    if (grp == 1 && rd < 2) { if (rd == 0) WAITV(12); else WAITV(12); }
    else if (grp == 1 && rd == 2) {
      WAITV(8);
      if (lane == 0) st_flag(ws + WS_FLAGO + blockIdx.x * 128u + w4 * 8u, tag);
      WAITV(5);
    }
    else if (grp == 1 && rd == 3) WAITV(5);
    else if (rd < 7) WAITV(4); else WAITV(0);
#pragma unroll
    for (unsigned u = 0; u < 2; ++u) {
      const unsigned po = ((4u * rd + 2u * u) & 7u) * 1024u;
      bw[2 * rd + u][0] = ldtr(bt0 + po, bt0 + po + 1024u); bw[2 * rd + u][1] = ldtr(bt1 + po, bt1 + po + 1024u);
    }
    if (rd < 6) { WAITL0(); dma_b4(4u * rd + 8u); }
  }
  STAMP(21);
  if (wid == 0) {
    const char* fp = ws + WS_FLAGO + (((blockIdx.x & ~56u) | (((lane >> 2) & 7u) << 3)) * 128u) + p4 * 8u;
    for (unsigned sp = 0; sp < SPIN_CAP; ++sp) {
      if (__all(ld_flag(fp) == tag)) break;
      __builtin_amdgcn_s_sleep(1);
    }
  }
  BAR();
  STAMP(22);
  const unsigned ah = (wid + hl) & 7u;
  auto dma_a1 = [&](unsigned i, unsigned jj) {
    const unsigned rbk = (kq + 1u + i) & 3u;
    dma16<2>(ldsb + (i & 1u) * 65536u + ah * 8192u + jj * 1024u, l16, srdWS, WS_OFRAG + ((b * 8u + 2u * rbk + (jj & 1u)) * 128u + (kq * 8u + ah) * 4u + (jj >> 1)) * 1024u);
  };
#pragma unroll
  for (unsigned jj = 0; jj < 8; ++jj) dma_a1(0, jj);
#pragma unroll
  for (unsigned jj = 0; jj < 8; ++jj) dma_a1(1, jj);
  WAITV(8); BARX();
  STAMP(23);
  const unsigned red = ldsb + 4u * SLOT;
  const unsigned abase = ldsb + grp * 32768u + l16;
  f32x4 prev[2];
#pragma unroll
  for (unsigned i = 0; i < 4; ++i) {
    unsigned aa[4];
#pragma unroll
    for (unsigned cc = 0; cc < 4; ++cc) { aa[cc] = abase + (i & 1u) * 65536u + ((prot + 8u * cc) & 31u) * 1024u; OPAQUE(aa[cc]); }
    bf16x8 af[32];
#define ARD(m) af[m] = ld128(aa[(m) >> 3] + 1024u * ((m) & 7))
    ARD(0); SB(); ARD(1); SB(); ARD(2); SB(); ARD(3); SB();
    f32x4 u0, u1;
    if (i > 0) { u0 = *(LAS f32x4*)(red + ((i - 1u) & 1u) * 16384u + (wid ^ 4u) * 2048u + l16); u1 = *(LAS f32x4*)(red + ((i - 1u) & 1u) * 16384u + (wid ^ 4u) * 2048u + 1024u + l16); }
    SB();
    f32x4 acc[2][2] = {};
#pragma unroll
    for (int m = 0; m < 32; ++m) {
      const int kk = m >> 1, qt = m & 1;
      acc[0][qt] = MFMA(bw[kk][0], af[m], acc[0][qt]);
      if (m + 4 < 32) ARD(m + 4);
      SB();
      acc[1][qt] = MFMA(bw[kk][1], af[m], acc[1][qt]);
      if (i > 0 && m == 2) {
        const unsigned dst = (blockIdx.x & ~6u) | (((kq + i) & 3u) << 1);
        const unsigned so = WS_SLAB + (dst * 4u + kq) * 16384u + wid * 2048u;
        st16_sc1(__builtin_bit_cast(u32x4, prev[0] + u0), l16, srdWS, so); st16_sc1(__builtin_bit_cast(u32x4, prev[1] + u1), l16, srdWS, so + 1024u);
      }
      if (i > 0 && i < 3 && m >= 4 && m < 20 && (m & 1) == 0) dma_a1(i + 1, (m - 4) >> 1);
      if (i > 0 && m == 22) {
        if (i < 3) WAITV(8); else WAITV(0);
        const unsigned dst = (blockIdx.x & ~6u) | (((kq + i) & 3u) << 1);
        if (lane == 0) st_flag(ws + WS_FLAGS + (dst * 4u + kq) * 128u + wid * 8u, tag);
      }
      SB();
    }
    {
      const unsigned rw = red + (i & 1u) * 16384u + wid * 2048u + l16;
      if (grp == 0) { *(LAS f32x4*)(rw) = acc[1][0]; *(LAS f32x4*)(rw + 1024u) = acc[1][1]; prev[0] = acc[0][0]; prev[1] = acc[0][1]; }
      else          { *(LAS f32x4*)(rw) = acc[0][0]; *(LAS f32x4*)(rw + 1024u) = acc[0][1]; prev[0] = acc[1][0]; prev[1] = acc[1][1]; }
    }
    if (i == 0) WAITV(0); else if (i < 3) WAITV(1);
    BAR();
    STAMP(24 + i);
  }
  {
    f32x4 a0 = prev[0] + *(LAS f32x4*)(red + 16384u + (wid ^ 4u) * 2048u + l16), a1 = prev[1] + *(LAS f32x4*)(red + 16384u + (wid ^ 4u) * 2048u + 1024u + l16);
    const unsigned src = (kq + 1u + (lane & 3u) % 3u) & 3u;
    const char* fp = ws + WS_FLAGS + (blockIdx.x * 4u + src) * 128u + wid * 8u;
    for (unsigned sp = 0; sp < SPIN_CAP; ++sp) {
      if (__all(ld_flag(fp) == tag)) break;
      __builtin_amdgcn_s_sleep(1);
    }
    asm volatile("" ::: "memory");
    STAMP(28);
#pragma unroll
    for (unsigned jj = 1; jj < 4; ++jj) {
      const unsigned so = WS_SLAB + (blockIdx.x * 4u + ((kq + jj) & 3u)) * 16384u + wid * 2048u;
      a0 += __builtin_bit_cast(f32x4, __builtin_amdgcn_raw_buffer_load_b128(srdWS, l16, so, 16));
      a1 += __builtin_bit_cast(f32x4, __builtin_amdgcn_raw_buffer_load_b128(srdWS, l16, so + 1024u, 16));
    }
    char* op = out + (size_t)(b * 128u + kq * 32u + c) * 2048u + hl * 256u + w4 * 64u + grp * 32u + g * 8u;
    *(u32x2*)op = u32x2{cvtpk(a0[0], a0[1]), cvtpk(a0[2], a0[3])};
    *(u32x2*)(op + 32768) = u32x2{cvtpk(a1[0], a1[1]), cvtpk(a1[2], a1[3])};
    if (h == 0 && tid == 0) st_flag(ws + WS_EPOCH + b * 128u, tag);
  }
  STAMP(29);
}

extern "C" void kernel_launch(void* const* d_in, const int* in_sizes, int n_in, void* d_out, int out_size, void* d_ws, size_t ws_size, hipStream_t stream) {
  (void)in_sizes; (void)n_in; (void)out_size; (void)ws_size;
  (void)hipFuncSetAttribute((const void*)mha_fused, hipFuncAttributeMaxDynamicSharedMemorySize, 163840);
  mha_fused<<<256, 512, 163840, stream>>>((const char*)d_in[0], (const char*)d_in[1], (const char*)d_in[2], (const char*)d_in[3], (const char*)d_in[4], (char*)d_out, (char*)d_ws);
}
```

```cpp
#include <hip/hip_runtime.h>
#include <hip/hip_bf16.h>
#include <cstdio>
#define STAMPS 1

typedef __bf16   bf16x8 __attribute__((ext_vector_type(8)));
typedef __bf16   bf16x4 __attribute__((ext_vector_type(4)));
typedef __bf16   bf16x2 __attribute__((ext_vector_type(2)));
typedef float    f32x4  __attribute__((ext_vector_type(4)));
typedef float    f32x2  __attribute__((ext_vector_type(2)));
typedef unsigned u32x4  __attribute__((ext_vector_type(4)));
typedef unsigned u32x2  __attribute__((ext_vector_type(2)));
typedef unsigned long long u64;
#define LAS __attribute__((address_space(3)))
#define GAS __attribute__((address_space(1)))
#define DI __device__ __forceinline__
#define MFMA(a, b, c) __builtin_amdgcn_mfma_f32_16x16x32_bf16((a), (b), (c), 0, 0, 0)
#define WAITV(n) asm volatile("s_waitcnt vmcnt(" #n ")" ::: "memory")
#define WAITL0() asm volatile("s_waitcnt lgkmcnt(0)" ::: "memory")
#define SB() __builtin_amdgcn_sched_barrier(0)
#define OPAQUE(x) asm volatile("" : "+v"(x))
#define BAR() do { asm volatile("s_waitcnt lgkmcnt(0)" ::: "memory"); __builtin_amdgcn_s_barrier(); asm volatile("" ::: "memory"); } while (0)
#define BARX() do { asm volatile("" ::: "memory"); __builtin_amdgcn_s_barrier(); asm volatile("" ::: "memory"); } while (0)
#define STAMP(i) do { const unsigned t_ = (unsigned)((wid & 1) ? __builtin_amdgcn_s_memtime() : wall_clock64()); tsv = lane == (unsigned)(i) ? t_ : tsv; } while (0)
#define KV_EARLY 7
#define PACE 0

constexpr int SQ = 128, DM = 1024, H_ = 32, SK = 1024, DH = 128;
constexpr unsigned SLOT = 32768u;
constexpr float QSCALE = 0.12751743082f;
constexpr float THR = 8.0f;
constexpr unsigned WS_OFRAG = 0u;
constexpr unsigned WS_SLAB  = 8u << 20;
constexpr unsigned WS_FLAGO = 24u << 20;
constexpr unsigned WS_FLAGS = WS_FLAGO + 32768u;
constexpr unsigned WS_EPOCH = WS_FLAGS + 131072u;
constexpr unsigned SPIN_CAP = 1u << 20;

DI unsigned rfl(unsigned x) { return __builtin_amdgcn_readfirstlane(x); }
DI unsigned cvtpk(float lo, float hi) { f32x2 v = {lo, hi}; bf16x2 b = __builtin_convertvector(v, bf16x2); return __builtin_bit_cast(unsigned, b); }
DI bf16x8 pack8(const f32x4& x, const f32x4& y) {
  u32x4 w = {cvtpk(x[0], x[1]), cvtpk(x[2], x[3]), cvtpk(y[0], y[1]), cvtpk(y[2], y[3])};
  return __builtin_bit_cast(bf16x8, w);
}
DI float fadd_s(float a, float b) { float r; asm("v_add_f32_e32 %0, %1, %2" : "=v"(r) : "v"(a), "v"(b)); return r; }
DI float ex2(float x) { return __builtin_amdgcn_exp2f(x); }
DI float red4_max(float x) {
  auto a = __builtin_amdgcn_permlane32_swap(__float_as_uint(x), __float_as_uint(x), false, false);
  x = fmaxf(__uint_as_float(a[0]), __uint_as_float(a[1]));
  auto b = __builtin_amdgcn_permlane16_swap(__float_as_uint(x), __float_as_uint(x), false, false);
  return fmaxf(__uint_as_float(b[0]), __uint_as_float(b[1]));
}
DI float red4_sum(float x) {
  auto a = __builtin_amdgcn_permlane32_swap(__float_as_uint(x), __float_as_uint(x), false, false);
  x = __uint_as_float(a[0]) + __uint_as_float(a[1]);
  auto b = __builtin_amdgcn_permlane16_swap(__float_as_uint(x), __float_as_uint(x), false, false);
  return __uint_as_float(b[0]) + __uint_as_float(b[1]);
}
template <int POL> DI void dma16(unsigned m0v, unsigned voff, __amdgpu_buffer_rsrc_t srd, unsigned soff) {
  if constexpr (POL == 1)      asm volatile("s_mov_b32 m0, %0\n\ts_nop 0\n\tbuffer_load_dwordx4 %1, %2, %3 offen nt lds" :: "s"(m0v), "v"(voff), "s"(srd), "s"(soff) : "memory");
  else if constexpr (POL == 2) asm volatile("s_mov_b32 m0, %0\n\ts_nop 0\n\tbuffer_load_dwordx4 %1, %2, %3 offen sc1 lds" :: "s"(m0v), "v"(voff), "s"(srd), "s"(soff) : "memory");
  else                         asm volatile("s_mov_b32 m0, %0\n\ts_nop 0\n\tbuffer_load_dwordx4 %1, %2, %3 offen lds" :: "s"(m0v), "v"(voff), "s"(srd), "s"(soff) : "memory");
}
DI void st16_sc1(u32x4 v, unsigned voff, __amdgpu_buffer_rsrc_t srd, unsigned soff) {
  asm volatile("buffer_store_dwordx4 %0, %1, %2, %3 offen sc1\n\ts_nop 1" :: "v"(v), "v"(voff), "s"(srd), "s"(soff) : "memory");
}
DI bf16x8 ld128(unsigned addr) { return *(LAS bf16x8*)addr; }
DI bf16x8 ldtr(unsigned a0, unsigned a1) {
  bf16x4 lo = __builtin_amdgcn_ds_read_tr16_b64_v4bf16((LAS bf16x4*)a0);
  bf16x4 hi = __builtin_amdgcn_ds_read_tr16_b64_v4bf16((LAS bf16x4*)a1);
  return __builtin_shufflevector(lo, hi, 0, 1, 2, 3, 4, 5, 6, 7);
}
DI unsigned fr(unsigned row) { return ((row >> 1) & 7u) ^ (((row >> 2) ^ (row >> 3)) & 1u); }
DI u64 ld_flag(const char* p) { return __hip_atomic_load((GAS u64*)p, __ATOMIC_RELAXED, __HIP_MEMORY_SCOPE_AGENT); }
DI void st_flag(char* p, u64 v) { __hip_atomic_store((GAS u64*)p, v, __ATOMIC_RELAXED, __HIP_MEMORY_SCOPE_AGENT); }
DI unsigned kaddr(unsigned m) { return m == 2u ? 16384u : 131072u + 16384u * m; }
DI unsigned vaddr(unsigned m) { return m == 0u ? 0u : 16384u + 16384u * m; }

__global__ __launch_bounds__(512, 2)
void mha_fused(const char* __restrict__ x, const char* __restrict__ kc, const char* __restrict__ vc, const char* __restrict__ wq,
               const char* __restrict__ wo, char* __restrict__ out, char* ws) {
  extern __shared__ __attribute__((aligned(16))) char lds[];
  const unsigned tid = threadIdx.x, lane = tid & 63u, wid = rfl(tid >> 6), grp = wid >> 2, w4 = wid & 3u;
  const unsigned c = lane & 15u, g = lane >> 4, q4 = c >> 2, p4 = lane & 3u, l16 = lane * 16u;
  const unsigned xg = blockIdx.x & 7u, idx = blockIdx.x >> 3, bl = idx >> 3, hl = idx & 7u, kq = xg >> 1;
  const unsigned b = 4u * (xg & 1u) + bl, h = 8u * kq + hl, unit = b * 32u + h;
  unsigned tsv = 0;
  STAMP(0);
  const unsigned ldsb = rfl((unsigned)(size_t)(LAS char*)lds);
  const __amdgpu_buffer_rsrc_t srdK = __builtin_amdgcn_make_buffer_rsrc((void*)(kc + (size_t)unit * (SK * DH * 2)), (short)0, SK * DH * 2, 0x00020000);
  const __amdgpu_buffer_rsrc_t srdV = __builtin_amdgcn_make_buffer_rsrc((void*)(vc + (size_t)unit * (SK * DH * 2)), (short)0, SK * DH * 2, 0x00020000);
  const unsigned rl = lane >> 3, ckr = ((lane & 7u) ^ fr(rl)) * 16u;
  const unsigned vk0 = rl * 256u + ckr, vk1 = rl * 256u + (ckr ^ 80u), vv = rl * 256u + (((lane & 7u) ^ (rl & 6u)) * 16u);
  auto dma_k = [&](unsigned t, unsigned la) {
    const unsigned m = ldsb + la + w4 * 4096u, so = t * 16384u + w4 * 4096u;
    dma16<1>(m, vk0, srdK, so);                  dma16<1>(m + 1024u, vk0, srdK, so + 128u);
    dma16<1>(m + 2048u, vk1, srdK, so + 2048u);  dma16<1>(m + 3072u, vk1, srdK, so + 2176u);
  };
  auto dma_v = [&](unsigned t, unsigned la) {
    const unsigned m = ldsb + la + w4 * 4096u, so = t * 16384u + w4 * 4096u;
    dma16<1>(m, vv, srdV, so);                  dma16<1>(m + 1024u, vv, srdV, so + 128u);
    dma16<1>(m + 2048u, vv, srdV, so + 2048u);  dma16<1>(m + 3072u, vv, srdV, so + 2176u);
  };

  u64 epoch;
  const char* epoch_p = ws + WS_EPOCH + b * 128u;
#define EPOCH_LOAD() asm volatile("s_load_dwordx2 %0, %1, 0x0" : "=s"(epoch) : "s"(epoch_p) : "memory")
#define EPOCH_WAIT() asm volatile("s_waitcnt lgkmcnt(0)" : "+s"(epoch) :: "memory")
  if (grp == 1) {
    const __amdgpu_buffer_rsrc_t srdX = __builtin_amdgcn_make_buffer_rsrc((void*)(x + (size_t)b * (SQ * DM * 2)), (short)0, SQ * DM * 2, 0x00020000);
    const __amdgpu_buffer_rsrc_t srdW = __builtin_amdgcn_make_buffer_rsrc((void*)(wq + h * 256u), (short)0, (int)(DM * H_ * DH * 2 - h * 256u), 0x00020000);
    const unsigned k0 = (2u * hl + 4u * bl) & 15u;
    const unsigned ckw = ((lane & 7u) ^ (rl & 2u)) * 16u;
    const unsigned vx0 = rl * 2048u + ckr, vx1 = rl * 2048u + (ckr ^ 80u), vw0 = rl * 8192u + ckw, vw1 = rl * 8192u + (ckw ^ 64u);
    auto dma_q = [&](unsigned i) {
      const unsigned kt = (k0 + i) & 15u;
      const unsigned m = ldsb + (i & 3u) * SLOT + w4 * 4096u, sx = w4 * 32u * 2048u + kt * 128u, sw = (kt * 64u + w4 * 16u) * 8192u;
      dma16<0>(m, vx0, srdX, sx);                    dma16<0>(m + 1024u, vx1, srdX, sx + 16384u);
      dma16<0>(m + 2048u, vx0, srdX, sx + 32768u);   dma16<0>(m + 3072u, vx1, srdX, sx + 49152u);
      dma16<0>(m + 16384u, vw0, srdW, sw);           dma16<0>(m + 17408u, vw0, srdW, sw + 128u);
      dma16<0>(m + 18432u, vw1, srdW, sw + 65536u);  dma16<0>(m + 19456u, vw1, srdW, sw + 65664u);
    };
    dma_q(0); dma_q(1); dma_q(2);
    EPOCH_LOAD();
    WAITV(8); EPOCH_WAIT(); BARX();
    for (unsigned i = 0; i < 13; ++i) {
      dma_q(i + 3);
      if (i == KV_EARLY) { dma_k(0, kaddr(0)); dma_k(1, kaddr(1)); }
      if (i == KV_EARLY || i == KV_EARLY + 1) WAITV(16); else WAITV(8);
      BARX();
    }
    dma_v(0, vaddr(0)); dma_k(2, kaddr(2));
    WAITV(8); BARX();
    dma_v(1, vaddr(1)); BARX();
  } else {
    const unsigned dh = w4 & 1u, qh = w4 >> 1;
    f32x4 acc[4][4] = {};
    unsigned xb[2], wb[4];
#pragma unroll
    for (unsigned s = 0; s < 2; ++s) xb[s] = ldsb + 8192u * qh + 1024u * (c >> 3) + 128u * (c & 7u) + 16u * ((4u * s + g) ^ fr(c));
#pragma unroll
    for (unsigned i = 0; i < 4; ++i) wb[i] = ldsb + 16384u + 2048u * g + 1024u * dh + 128u * q4 + 16u * ((2u * i + (p4 >> 1)) ^ ((q4 & 2u) | ((g & 1u) << 2))) + 8u * (p4 & 1u);
    EPOCH_LOAD(); EPOCH_WAIT();
    BARX();
    STAMP(1);
    bf16x8 xf[2][4], wf[2][4];
    unsigned xa[2], wa[4];
#pragma unroll
    for (int i = 0; i < 4; ++i) { wa[i] = wb[i]; wf[0][i] = ldtr(wa[i], wa[i] + 512u); xf[0][i] = ld128(xb[0] + 2048u * i); }
    xa[0] = xb[0]; xa[1] = xb[1];
    for (unsigned it = 0; it < 16; ++it) {
      const unsigned nso = ((it + 1u) & 3u) * SLOT;
      SB();
#pragma unroll
      for (unsigned s = 0; s < 2; ++s) {
        if (s == 1) {
#pragma unroll
          for (int i = 0; i < 4; ++i) { wa[i] = wb[i] + nso; OPAQUE(wa[i]); }
          xa[0] = xb[0] + nso; OPAQUE(xa[0]);
        }
#pragma unroll
        for (unsigned m = 0; m < 16; ++m) {
          const unsigned i = m >> 2, j = m & 3;
          acc[i][j] = MFMA(wf[s][i], xf[s][j], acc[i][j]);
          if (m < 4) xf[s ^ 1][m] = ld128(xa[s ^ 1] + 2048u * m);
          if (m >= 4 && m < 12 && (m & 1) == 0) { const unsigned n = (m - 4) >> 1; wf[s ^ 1][n] = ldtr(wa[n] + 8192u * (s ^ 1), wa[n] + 8192u * (s ^ 1) + 512u); }
          SB();
        }
        if (s == 0) { xa[1] = xb[1] + nso; OPAQUE(xa[1]); }
      }
      if (it != 14) BARX();
      STAMP(32 + it);
    }
#pragma unroll
    for (unsigned i = 0; i < 4; ++i)
#pragma unroll
      for (unsigned j = 0; j < 4; ++j) {
        const u32x2 w = {cvtpk(acc[i][j][0] * QSCALE, acc[i][j][1] * QSCALE), cvtpk(acc[i][j][2] * QSCALE, acc[i][j][3] * QSCALE)};
        *(LAS u32x2*)(ldsb + 3u * SLOT + ((4u * qh + j) * 4u + 2u * dh + (i >> 1)) * 1024u + (16u * (2u * (i & 1u) + (g >> 1)) + c) * 16u + 8u * (g & 1u)) = w;
      }
  }
  STAMP(2);
  BAR();
  bf16x8 qf[2][4];
#pragma unroll
  for (int s = 0; s < 8; ++s) qf[s >> 2][s & 3] = ld128(ldsb + 3u * SLOT + w4 * 8192u + s * 1024u + l16);
  STAMP(3);

  const __amdgpu_buffer_rsrc_t srdB = __builtin_amdgcn_make_buffer_rsrc((void*)(wo + (size_t)(kq * 1024u + grp * 512u) * 2048u + hl * 256u + w4 * 64u), (short)0, 512 * 2048 - 1984, 0x00020000);
  const unsigned vB = (lane >> 2) * 2048u + ((p4 ^ ((g & 1u) << 1)) * 16u);
  const unsigned prot = ((hl + bl) & 3u) * 8u;
  const unsigned bstage = ldsb + 65536u + wid * 8192u;
  auto dma_b4 = [&](unsigned p0) {
#pragma unroll
    for (unsigned j = 0; j < 4; ++j) dma16<0>(bstage + ((p0 + j) & 7u) * 1024u, vB, srdB, ((prot + p0 + j) & 31u) * 32768u);
  };

  unsigned kb2[2], vb4[4];
#pragma unroll
  for (unsigned s = 0; s < 2; ++s) kb2[s] = ldsb + 2048u * (c >> 3) + 128u * (c & 7u) + 16u * ((4u * s + g) ^ fr(c));
#pragma unroll
  for (unsigned i = 0; i < 4; ++i) vb4[i] = ldsb + 2048u * (g >> 1) + 128u * (4u * (g & 1u) + q4) + 16u * ((2u * i + (p4 >> 1)) ^ ((q4 & 2u) | ((g & 1u) << 2))) + 8u * (p4 & 1u);
  f32x4 o[8][2] = {};
  f32x4 negm[2] = {};
  float mref[2] = {0.f, 0.f}, ls[2][2] = {{0.f, 0.f}, {0.f, 0.f}};
  if (grp == 1) { WAITV(8); BAR(); }
  unsigned jm = grp;
  for (unsigned it = 0; it < 8; ++it) {
    const unsigned j = 2u * it + grp;
    const unsigned jm1 = jm == 2u ? 0u : jm + 1u;
    f32x4 s[4][2];
    {
      unsigned ka[2];
      const unsigned so = kaddr(jm);
#pragma unroll
      for (int i = 0; i < 2; ++i) { ka[i] = kb2[i] + so; OPAQUE(ka[i]); }
      bf16x8 kf[16];
#define KRD(m) kf[m] = ld128(ka[(m) & 1] + 4096u * ((m) >> 2) + 1024u * (((m) >> 1) & 1))
      KRD(0); SB(); KRD(1); SB(); KRD(2); SB(); KRD(3); SB(); KRD(4);
      SB();
      float mx[2];
#pragma unroll
      for (int m = 0; m < 16; ++m) {
        const int kt = m >> 2, ds = m & 3;
#pragma unroll
        for (int qt = 0; qt < 2; ++qt) {
          s[kt][qt] = MFMA(kf[m], qf[qt][ds], ds == 0 ? negm[qt] : s[kt][qt]);
          if (qt == 0 && m + 5 < 16) KRD(m + 5);
          if (kt > 0 && ds >= 2) {
            const int e = 2 * (ds - 2);
            if (kt == 1 && ds == 2) mx[qt] = fmaxf(s[0][qt][0], s[0][qt][1]);
            else mx[qt] = fmaxf(fmaxf(mx[qt], s[kt - 1][qt][e]), s[kt - 1][qt][e + 1]);
          }
          SB();
        }
        if (PACE && ds == 3 && j < 14) __builtin_amdgcn_s_sleep(PACE);
      }
#pragma unroll
      for (int qt = 0; qt < 2; ++qt) { mx[qt] = fmaxf(fmaxf(mx[qt], s[3][qt][0]), s[3][qt][1]); mx[qt] = fmaxf(fmaxf(mx[qt], s[3][qt][2]), s[3][qt][3]); }
      if (it == 0 || __any(fmaxf(mx[0], mx[1]) > THR)) {
#pragma unroll
        for (int qt = 0; qt < 2; ++qt) {
          float dl = red4_max(mx[qt]);
          if (it != 0) {
            dl = fmaxf(dl, 0.f);
            const float f = ex2(-dl);
            ls[qt][0] *= f; ls[qt][1] *= f;
#pragma unroll
            for (int dt = 0; dt < 8; ++dt) o[dt][qt] *= f;
          }
          mref[qt] += dl;
#pragma unroll
          for (int kt = 0; kt < 4; ++kt) s[kt][qt] -= dl;
          negm[qt] = f32x4{-mref[qt], -mref[qt], -mref[qt], -mref[qt]};
        }
      }
    }
    bf16x8 pf[2][2];
#pragma unroll
    for (int qt = 0; qt < 2; ++qt)
#pragma unroll
      for (int e = 0; e < 4; ++e) { s[0][qt][e] = ex2(s[0][qt][e]); s[1][qt][e] = ex2(s[1][qt][e]); }
    SB();
    pf[0][0] = pack8(s[0][0], s[1][0]); pf[0][1] = pack8(s[0][1], s[1][1]);
    OPAQUE(pf[0][0]); OPAQUE(pf[0][1]);
    SB();
#pragma unroll
    for (int e = 0; e < 4; ++e)
#pragma unroll
      for (int qt = 0; qt < 2; ++qt) { ls[qt][0] = fadd_s(ls[qt][0], s[0][qt][e]); ls[qt][1] = fadd_s(ls[qt][1], s[1][qt][e]); }
    SB();
    if (it == 3) STAMP(48); if (it == 7) STAMP(52);
    if (j - 1u < 14u) { dma_v(j + 1u, vaddr(jm1)); WAITV(4); }
    else if (j == 15u) { dma_b4(0); dma_b4(4); }
    if (it == 3) STAMP(49); if (it == 7) STAMP(53);
    BARX();
    STAMP(4 + 2 * it);
    {
      if (j <= 12u) dma_k(j + 3u, kaddr(jm));
      else if (j == 14u) { dma_b4(0); dma_b4(4); }
      if (it == 3) STAMP(50); if (it == 7) STAMP(54);
      unsigned va[4];
      const unsigned so = vaddr(jm);
#pragma unroll
      for (int i = 0; i < 4; ++i) { va[i] = vb4[i] + so; OPAQUE(va[i]); }
      bf16x8 vf[16];
#define VRD(m) vf[m] = ldtr(va[(m) & 3] + 8192u * ((m) >> 3) + 1024u * (((m) >> 2) & 1), va[(m) & 3] + 8192u * ((m) >> 3) + 1024u * (((m) >> 2) & 1) + 4096u)
      VRD(0); SB(); VRD(1); SB(); VRD(2); SB(); VRD(3);
      SB();
#pragma unroll
      for (int m = 0; m < 16; ++m) {
        const int ks = m >> 3, dt = m & 7;
#pragma unroll
        for (int qt = 0; qt < 2; ++qt) {
          o[dt][qt] = MFMA(vf[m], pf[ks][qt], o[dt][qt]);
          if (qt == 0 && m + 4 < 16) VRD(m + 4);
          if (ks == 0) { const int kt = 2 + (dt >> 2), e = dt & 3; s[kt][qt][e] = ex2(s[kt][qt][e]); if (dt == 7) pf[1][qt] = pack8(s[2][qt], s[3][qt]); }
          else { const int kt = 2 + (dt >> 2), e = dt & 3; ls[qt][dt & 1] = fadd_s(ls[qt][dt & 1], s[kt][qt][e]); }
          SB();
        }
        if (PACE && (dt & 3) == 3 && j < 14) __builtin_amdgcn_s_sleep(PACE);
      }
      if (it == 3) STAMP(51); if (it == 7) STAMP(55);
      if (j <= 12u) WAITV(4); else if (j == 13u) WAITV(0); else if (j == 14u) WAITV(8);
    }
    jm = jm == 0u ? 2u : jm - 1u;
    BARX();
    STAMP(5 + 2 * it);
  }

  const __amdgpu_buffer_rsrc_t srdWS = __builtin_amdgcn_make_buffer_rsrc((void*)ws, (short)0, (int)WS_FLAGO, 0x00020000);
  const u64 tag = epoch + 1ull;
  const unsigned xo0 = ldsb + 131072u + w4 * 8192u + l16, xo1 = ldsb + 32768u + w4 * 8192u + l16, xml = ldsb + 16384u + w4 * 1024u + l16;
  const float lt0 = red4_sum(ls[0][0] + ls[0][1]), lt1 = red4_sum(ls[1][0] + ls[1][1]);
  if (grp == 0) {
#pragma unroll
    for (int dt = 0; dt < 4; ++dt)
#pragma unroll
      for (int qt = 0; qt < 2; ++qt) { *(LAS f32x4*)(xo0 + (2 * dt + qt) * 1024u) = o[dt][qt]; *(LAS f32x4*)(xo1 + (2 * dt + qt) * 1024u) = o[4 + dt][qt]; }
    *(LAS f32x4*)xml = f32x4{mref[0], lt0, mref[1], lt1};
    BAR();
  } else {
    const f32x4 ot = *(LAS f32x4*)xml;
    float wa_[2], wb_[2];
    {
      const float M0 = fmaxf(mref[0], ot[0]), M1 = fmaxf(mref[1], ot[2]);
      const float a0 = ex2(mref[0] - M0), b0 = ex2(ot[0] - M0), a1 = ex2(mref[1] - M1), b1 = ex2(ot[2] - M1);
      const float r0 = __builtin_amdgcn_rcpf(a0 * lt0 + b0 * ot[1]), r1 = __builtin_amdgcn_rcpf(a1 * lt1 + b1 * ot[3]);
      wa_[0] = a0 * r0; wb_[0] = b0 * r0; wa_[1] = a1 * r1; wb_[1] = b1 * r1;
    }
#pragma unroll
    for (int s2 = 0; s2 < 4; ++s2)
#pragma unroll
      for (int qt = 0; qt < 2; ++qt) {
        const unsigned xo = s2 < 2 ? xo0 : xo1;
        const f32x4 u = *(LAS f32x4*)(xo + (4 * (s2 & 1) + qt) * 1024u), v = *(LAS f32x4*)(xo + (4 * (s2 & 1) + 2 + qt) * 1024u);
        const bf16x8 f = pack8(o[2 * s2][qt] * wa_[qt] + u * wb_[qt], o[2 * s2 + 1][qt] * wa_[qt] + v * wb_[qt]);
        st16_sc1(__builtin_bit_cast(u32x4, f), l16, srdWS, WS_OFRAG + ((b * 8u + 2u * w4 + qt) * 128u + h * 4u + s2) * 1024u);
      }
  }
  STAMP(20);
  bf16x8 bw[16][2];
  const unsigned bt0 = bstage + 64u * (4u * g + q4) + 32u * (g & 1u) + 8u * p4, bt1 = bt0 ^ 32u;
#pragma unroll
  for (unsigned rd = 0; rd < 8; ++rd) {
    if (grp == 1 && rd < 2) { if (rd == 0) WAITV(12); else WAITV(12); }
    else if (grp == 1 && rd == 2) {
      WAITV(8);
      if (lane == 0) st_flag(ws + WS_FLAGO + blockIdx.x * 128u + w4 * 8u, tag);
      WAITV(5);
    }
    else if (grp == 1 && rd == 3) WAITV(5);
    else if (rd < 7) WAITV(4); else WAITV(0);
#pragma unroll
    for (unsigned u = 0; u < 2; ++u) {
      const unsigned po = ((4u * rd + 2u * u) & 7u) * 1024u;
      bw[2 * rd + u][0] = ldtr(bt0 + po, bt0 + po + 1024u); bw[2 * rd + u][1] = ldtr(bt1 + po, bt1 + po + 1024u);
    }
    if (rd < 6) { WAITL0(); dma_b4(4u * rd + 8u); }
  }
  STAMP(21);
  if (wid == 0) {
    const char* fp = ws + WS_FLAGO + (((blockIdx.x & ~56u) | (((lane >> 2) & 7u) << 3)) * 128u) + p4 * 8u;
    for (unsigned sp = 0; sp < SPIN_CAP; ++sp) {
      if (__all(ld_flag(fp) == tag)) break;
      __builtin_amdgcn_s_sleep(1);
    }
  }
  BAR();
  STAMP(22);
  const unsigned ah = (wid + hl) & 7u;
  auto dma_a1 = [&](unsigned i, unsigned jj) {
    const unsigned rbk = (kq + 1u + i) & 3u;
    dma16<2>(ldsb + (i & 1u) * 65536u + ah * 8192u + jj * 1024u, l16, srdWS, WS_OFRAG + ((b * 8u + 2u * rbk + (jj & 1u)) * 128u + (kq * 8u + ah) * 4u + (jj >> 1)) * 1024u);
  };
#pragma unroll
  for (unsigned jj = 0; jj < 8; ++jj) dma_a1(0, jj);
#pragma unroll
  for (unsigned jj = 0; jj < 8; ++jj) dma_a1(1, jj);
  WAITV(8); BARX();
  STAMP(23);
  const unsigned red = ldsb + 4u * SLOT;
  const unsigned abase = ldsb + grp * 32768u + l16;
  f32x4 prev[2];
#pragma unroll
  for (unsigned i = 0; i < 4; ++i) {
    unsigned aa[4];
#pragma unroll
    for (unsigned cc = 0; cc < 4; ++cc) { aa[cc] = abase + (i & 1u) * 65536u + ((prot + 8u * cc) & 31u) * 1024u; OPAQUE(aa[cc]); }
    bf16x8 af[32];
#define ARD(m) af[m] = ld128(aa[(m) >> 3] + 1024u * ((m) & 7))
    ARD(0); SB(); ARD(1); SB(); ARD(2); SB(); ARD(3); SB();
    f32x4 u0, u1;
    if (i > 0) { u0 = *(LAS f32x4*)(red + ((i - 1u) & 1u) * 16384u + (wid ^ 4u) * 2048u + l16); u1 = *(LAS f32x4*)(red + ((i - 1u) & 1u) * 16384u + (wid ^ 4u) * 2048u + 1024u + l16); }
    SB();
    f32x4 acc[2][2] = {};
#pragma unroll
    for (int m = 0; m < 32; ++m) {
      const int kk = m >> 1, qt = m & 1;
      acc[0][qt] = MFMA(bw[kk][0], af[m], acc[0][qt]);
      if (m + 4 < 32) ARD(m + 4);
      SB();
      acc[1][qt] = MFMA(bw[kk][1], af[m], acc[1][qt]);
      if (i > 0 && m == 2) {
        const unsigned dst = (blockIdx.x & ~6u) | (((kq + i) & 3u) << 1);
        const unsigned so = WS_SLAB + (dst * 4u + kq) * 16384u + wid * 2048u;
        st16_sc1(__builtin_bit_cast(u32x4, prev[0] + u0), l16, srdWS, so); st16_sc1(__builtin_bit_cast(u32x4, prev[1] + u1), l16, srdWS, so + 1024u);
      }
      if (i > 0 && i < 3 && m >= 4 && m < 20 && (m & 1) == 0) dma_a1(i + 1, (m - 4) >> 1);
      if (i > 0 && m == 22) {
        if (i < 3) WAITV(8); else WAITV(0);
        const unsigned dst = (blockIdx.x & ~6u) | (((kq + i) & 3u) << 1);
        if (lane == 0) st_flag(ws + WS_FLAGS + (dst * 4u + kq) * 128u + wid * 8u, tag);
      }
      SB();
    }
    {
      const unsigned rw = red + (i & 1u) * 16384u + wid * 2048u + l16;
      if (grp == 0) { *(LAS f32x4*)(rw) = acc[1][0]; *(LAS f32x4*)(rw + 1024u) = acc[1][1]; prev[0] = acc[0][0]; prev[1] = acc[0][1]; }
      else          { *(LAS f32x4*)(rw) = acc[0][0]; *(LAS f32x4*)(rw + 1024u) = acc[0][1]; prev[0] = acc[1][0]; prev[1] = acc[1][1]; }
    }
    if (i == 0) WAITV(0); else if (i < 3) WAITV(1);
    BAR();
    STAMP(24 + i);
  }
  {
    f32x4 a0 = prev[0] + *(LAS f32x4*)(red + 16384u + (wid ^ 4u) * 2048u + l16), a1 = prev[1] + *(LAS f32x4*)(red + 16384u + (wid ^ 4u) * 2048u + 1024u + l16);
    const unsigned src = (kq + 1u + (lane & 3u) % 3u) & 3u;
    const char* fp = ws + WS_FLAGS + (blockIdx.x * 4u + src) * 128u + wid * 8u;
    for (unsigned sp = 0; sp < SPIN_CAP; ++sp) {
      if (__all(ld_flag(fp) == tag)) break;
      __builtin_amdgcn_s_sleep(1);
    }
    asm volatile("" ::: "memory");
    STAMP(28);
#pragma unroll
    for (unsigned jj = 1; jj < 4; ++jj) {
      const unsigned so = WS_SLAB + (blockIdx.x * 4u + ((kq + jj) & 3u)) * 16384u + wid * 2048u;
      a0 += __builtin_bit_cast(f32x4, __builtin_amdgcn_raw_buffer_load_b128(srdWS, l16, so, 16));
      a1 += __builtin_bit_cast(f32x4, __builtin_amdgcn_raw_buffer_load_b128(srdWS, l16, so + 1024u, 16));
    }
    char* op = out + (size_t)(b * 128u + kq * 32u + c) * 2048u + hl * 256u + w4 * 64u + grp * 32u + g * 8u;
    *(u32x2*)op = u32x2{cvtpk(a0[0], a0[1]), cvtpk(a0[2], a0[3])};
    *(u32x2*)(op + 32768) = u32x2{cvtpk(a1[0], a1[1]), cvtpk(a1[2], a1[3])};
    if (h == 0 && tid == 0) st_flag(ws + WS_EPOCH + b * 128u, tag);
  }
  STAMP(29);
  ((unsigned*)(ws + (32u << 20)))[(blockIdx.x * 8 + wid) * 64 + lane] = tsv;
}

__global__ void dump(const unsigned* t) {
  if (threadIdx.x == 0) for (int blk = 0; blk < 256; blk += 85) { const unsigned* p = t + blk * 8 * 64; printf("blk %d:", blk); for (int j = 1; j < 30; ++j) printf(" %d", (int)(p[j] - p[0])); printf("\n"); }
}
extern "C" void kernel_launch(void* const* d_in, const int* in_sizes, int n_in, void* d_out, int out_size, void* d_ws, size_t ws_size, hipStream_t stream) {
  fprintf(stderr, "ptrs x %p k %p v %p wq %p wo %p out %p ws %p wsz %zu\n", d_in[0], d_in[1], d_in[2], d_in[3], d_in[4], d_out, d_ws, ws_size);
  (void)in_sizes; (void)n_in; (void)out_size; (void)ws_size;
  (void)hipFuncSetAttribute((const void*)mha_fused, hipFuncAttributeMaxDynamicSharedMemorySize, 163840);
  mha_fused<<<256, 512, 163840, stream>>>((const char*)d_in[0], (const char*)d_in[1], (const char*)d_in[2], (const char*)d_in[3], (const char*)d_in[4], (char*)d_out, (char*)d_ws);
  dump<<<1, 64, 0, stream>>>((const unsigned*)((char*)d_ws + (32u << 20)));
}
```

```cpp
#include <hip/hip_runtime.h>
#include <hip/hip_bf16.h>
#define STAMPS 1

typedef __bf16   bf16x8 __attribute__((ext_vector_type(8)));
typedef __bf16   bf16x4 __attribute__((ext_vector_type(4)));
typedef __bf16   bf16x2 __attribute__((ext_vector_type(2)));
typedef float    f32x4  __attribute__((ext_vector_type(4)));
typedef float    f32x2  __attribute__((ext_vector_type(2)));
typedef unsigned u32x4  __attribute__((ext_vector_type(4)));
typedef unsigned u32x2  __attribute__((ext_vector_type(2)));
typedef unsigned long long u64;
#define LAS __attribute__((address_space(3)))
#define GAS __attribute__((address_space(1)))
#define DI __device__ __forceinline__
#define MFMA(a, b, c) __builtin_amdgcn_mfma_f32_16x16x32_bf16((a), (b), (c), 0, 0, 0)
#define WAITV(n) asm volatile("s_waitcnt vmcnt(" #n ")" ::: "memory")
#define WAITL0() asm volatile("s_waitcnt lgkmcnt(0)" ::: "memory")
#define SB() __builtin_amdgcn_sched_barrier(0)
#define OPAQUE(x) asm volatile("" : "+v"(x))
#define BAR() do { asm volatile("s_waitcnt lgkmcnt(0)" ::: "memory"); __builtin_amdgcn_s_barrier(); asm volatile("" ::: "memory"); } while (0)
#define BARX() do { asm volatile("" ::: "memory"); __builtin_amdgcn_s_barrier(); asm volatile("" ::: "memory"); } while (0)
#define STAMP(i) do { const unsigned t_ = (unsigned)((wid & 1) ? __builtin_amdgcn_s_memtime() : wall_clock64()); tsv = lane == (unsigned)(i) ? t_ : tsv; } while (0)
#define KV_EARLY 7
#define PACE 0

constexpr int SQ = 128, DM = 1024, H_ = 32, SK = 1024, DH = 128;
constexpr unsigned SLOT = 32768u;
constexpr float QSCALE = 0.12751743082f;
constexpr float THR = 8.0f;
constexpr unsigned WS_OFRAG = 0u;
constexpr unsigned WS_SLAB  = 8u << 20;
constexpr unsigned WS_FLAGO = 24u << 20;
constexpr unsigned WS_FLAGS = WS_FLAGO + 32768u;
constexpr unsigned WS_EPOCH = WS_FLAGS + 131072u;
constexpr unsigned SPIN_CAP = 1u << 20;

DI unsigned rfl(unsigned x) { return __builtin_amdgcn_readfirstlane(x); }
DI unsigned cvtpk(float lo, float hi) { f32x2 v = {lo, hi}; bf16x2 b = __builtin_convertvector(v, bf16x2); return __builtin_bit_cast(unsigned, b); }
DI bf16x8 pack8(const f32x4& x, const f32x4& y) {
  u32x4 w = {cvtpk(x[0], x[1]), cvtpk(x[2], x[3]), cvtpk(y[0], y[1]), cvtpk(y[2], y[3])};
  return __builtin_bit_cast(bf16x8, w);
}
DI float fadd_s(float a, float b) { float r; asm("v_add_f32_e32 %0, %1, %2" : "=v"(r) : "v"(a), "v"(b)); return r; }
DI float ex2(float x) { return __builtin_amdgcn_exp2f(x); }
DI float red4_max(float x) {
  auto a = __builtin_amdgcn_permlane32_swap(__float_as_uint(x), __float_as_uint(x), false, false);
  x = fmaxf(__uint_as_float(a[0]), __uint_as_float(a[1]));
  auto b = __builtin_amdgcn_permlane16_swap(__float_as_uint(x), __float_as_uint(x), false, false);
  return fmaxf(__uint_as_float(b[0]), __uint_as_float(b[1]));
}
DI float red4_sum(float x) {
  auto a = __builtin_amdgcn_permlane32_swap(__float_as_uint(x), __float_as_uint(x), false, false);
  x = __uint_as_float(a[0]) + __uint_as_float(a[1]);
  auto b = __builtin_amdgcn_permlane16_swap(__float_as_uint(x), __float_as_uint(x), false, false);
  return __uint_as_float(b[0]) + __uint_as_float(b[1]);
}
template <int POL> DI void dma16(unsigned m0v, unsigned voff, __amdgpu_buffer_rsrc_t srd, unsigned soff) {
  if constexpr (POL == 1)      asm volatile("s_mov_b32 m0, %0\n\ts_nop 0\n\tbuffer_load_dwordx4 %1, %2, %3 offen nt lds" :: "s"(m0v), "v"(voff), "s"(srd), "s"(soff) : "memory");
  else if constexpr (POL == 2) asm volatile("s_mov_b32 m0, %0\n\ts_nop 0\n\tbuffer_load_dwordx4 %1, %2, %3 offen sc1 lds" :: "s"(m0v), "v"(voff), "s"(srd), "s"(soff) : "memory");
  else                         asm volatile("s_mov_b32 m0, %0\n\ts_nop 0\n\tbuffer_load_dwordx4 %1, %2, %3 offen lds" :: "s"(m0v), "v"(voff), "s"(srd), "s"(soff) : "memory");
}
DI void st16_sc1(u32x4 v, unsigned voff, __amdgpu_buffer_rsrc_t srd, unsigned soff) {
  asm volatile("buffer_store_dwordx4 %0, %1, %2, %3 offen sc1\n\ts_nop 1" :: "v"(v), "v"(voff), "s"(srd), "s"(soff) : "memory");
}
DI bf16x8 ld128(unsigned addr) { return *(LAS bf16x8*)addr; }
DI bf16x8 ldtr(unsigned a0, unsigned a1) {
  bf16x4 lo = __builtin_amdgcn_ds_read_tr16_b64_v4bf16((LAS bf16x4*)a0);
  bf16x4 hi = __builtin_amdgcn_ds_read_tr16_b64_v4bf16((LAS bf16x4*)a1);
  return __builtin_shufflevector(lo, hi, 0, 1, 2, 3, 4, 5, 6, 7);
}
DI unsigned fr(unsigned row) { return ((row >> 1) & 7u) ^ (((row >> 2) ^ (row >> 3)) & 1u); }
DI u64 ld_flag(const char* p) { return __hip_atomic_load((GAS u64*)p, __ATOMIC_RELAXED, __HIP_MEMORY_SCOPE_AGENT); }
DI void st_flag(char* p, u64 v) { __hip_atomic_store((GAS u64*)p, v, __ATOMIC_RELAXED, __HIP_MEMORY_SCOPE_AGENT); }
DI unsigned kaddr(unsigned m) { return m == 2u ? 16384u : 131072u + 16384u * m; }
DI unsigned vaddr(unsigned m) { return m == 0u ? 0u : 16384u + 16384u * m; }

__global__ __launch_bounds__(512, 2)
void mha_fused(const char* __restrict__ x, const char* __restrict__ kc, const char* __restrict__ vc, const char* __restrict__ wq,
               const char* __restrict__ wo, char* __restrict__ out, char* ws) {
  extern __shared__ __attribute__((aligned(16))) char lds[];
  const unsigned tid = threadIdx.x, lane = tid & 63u, wid = rfl(tid >> 6), grp = wid >> 2, w4 = wid & 3u;
  const unsigned c = lane & 15u, g = lane >> 4, q4 = c >> 2, p4 = lane & 3u, l16 = lane * 16u;
  const unsigned xg = blockIdx.x & 7u, idx = blockIdx.x >> 3, bl = idx >> 3, hl = idx & 7u, kq = xg >> 1;
  const unsigned b = 4u * (xg & 1u) + bl, h = 8u * kq + hl, unit = b * 32u + h;
  unsigned tsv = 0;
  STAMP(0);
  const unsigned ldsb = rfl((unsigned)(size_t)(LAS char*)lds);
  const __amdgpu_buffer_rsrc_t srdK = __builtin_amdgcn_make_buffer_rsrc((void*)(kc + (size_t)unit * (SK * DH * 2)), (short)0, SK * DH * 2, 0x00020000);
  const __amdgpu_buffer_rsrc_t srdV = __builtin_amdgcn_make_buffer_rsrc((void*)(vc + (size_t)unit * (SK * DH * 2)), (short)0, SK * DH * 2, 0x00020000);
  const unsigned rl = lane >> 3, ckr = ((lane & 7u) ^ fr(rl)) * 16u;
  const unsigned vk0 = rl * 256u + ckr, vk1 = rl * 256u + (ckr ^ 80u), vv = rl * 256u + (((lane & 7u) ^ (rl & 6u)) * 16u);
  auto dma_k = [&](unsigned t, unsigned la) {
    const unsigned m = ldsb + la + w4 * 4096u, so = t * 16384u + w4 * 4096u;
    dma16<1>(m, vk0, srdK, so);                  dma16<1>(m + 1024u, vk0, srdK, so + 128u);
    dma16<1>(m + 2048u, vk1, srdK, so + 2048u);  dma16<1>(m + 3072u, vk1, srdK, so + 2176u);
  };
  auto dma_v = [&](unsigned t, unsigned la) {
    const unsigned m = ldsb + la + w4 * 4096u, so = t * 16384u + w4 * 4096u;
    dma16<1>(m, vv, srdV, so);                  dma16<1>(m + 1024u, vv, srdV, so + 128u);
    dma16<1>(m + 2048u, vv, srdV, so + 2048u);  dma16<1>(m + 3072u, vv, srdV, so + 2176u);
  };

  u64 epoch;
  const char* epoch_p = ws + WS_EPOCH + b * 128u;
#define EPOCH_LOAD() asm volatile("s_load_dwordx2 %0, %1, 0x0" : "=s"(epoch) : "s"(epoch_p) : "memory")
#define EPOCH_WAIT() asm volatile("s_waitcnt lgkmcnt(0)" : "+s"(epoch) :: "memory")
  if (grp == 1) {
    const __amdgpu_buffer_rsrc_t srdX = __builtin_amdgcn_make_buffer_rsrc((void*)(x + (size_t)b * (SQ * DM * 2)), (short)0, SQ * DM * 2, 0x00020000);
    const __amdgpu_buffer_rsrc_t srdW = __builtin_amdgcn_make_buffer_rsrc((void*)(wq + h * 256u), (short)0, (int)(DM * H_ * DH * 2 - h * 256u), 0x00020000);
    const unsigned k0 = (2u * hl + 4u * bl) & 15u;
    const unsigned ckw = ((lane & 7u) ^ (rl & 2u)) * 16u;
    const unsigned vx0 = rl * 2048u + ckr, vx1 = rl * 2048u + (ckr ^ 80u), vw0 = rl * 8192u + ckw, vw1 = rl * 8192u + (ckw ^ 64u);
    auto dma_q = [&](unsigned i) {
      const unsigned kt = (k0 + i) & 15u;
      const unsigned m = ldsb + (i & 3u) * SLOT + w4 * 4096u, sx = w4 * 32u * 2048u + kt * 128u, sw = (kt * 64u + w4 * 16u) * 8192u;
      dma16<0>(m, vx0, srdX, sx);                    dma16<0>(m + 1024u, vx1, srdX, sx + 16384u);
      dma16<0>(m + 2048u, vx0, srdX, sx + 32768u);   dma16<0>(m + 3072u, vx1, srdX, sx + 49152u);
      dma16<0>(m + 16384u, vw0, srdW, sw);           dma16<0>(m + 17408u, vw0, srdW, sw + 128u);
      dma16<0>(m + 18432u, vw1, srdW, sw + 65536u);  dma16<0>(m + 19456u, vw1, srdW, sw + 65664u);
    };
    dma_q(0); dma_q(1); dma_q(2);
    EPOCH_LOAD();
    WAITV(8); EPOCH_WAIT(); BARX();
    for (unsigned i = 0; i < 13; ++i) {
      dma_q(i + 3);
      if (i == KV_EARLY) { dma_k(0, kaddr(0)); dma_k(1, kaddr(1)); }
      if (i == KV_EARLY || i == KV_EARLY + 1) WAITV(16); else WAITV(8);
      BARX();
    }
    dma_v(0, vaddr(0)); dma_k(2, kaddr(2));
    WAITV(8); BARX();
    dma_v(1, vaddr(1)); BARX();
  } else {
    const unsigned dh = w4 & 1u, qh = w4 >> 1;
    f32x4 acc[4][4] = {};
    unsigned xb[2], wb[4];
#pragma unroll
    for (unsigned s = 0; s < 2; ++s) xb[s] = ldsb + 8192u * qh + 1024u * (c >> 3) + 128u * (c & 7u) + 16u * ((4u * s + g) ^ fr(c));
#pragma unroll
    for (unsigned i = 0; i < 4; ++i) wb[i] = ldsb + 16384u + 2048u * g + 1024u * dh + 128u * q4 + 16u * ((2u * i + (p4 >> 1)) ^ ((q4 & 2u) | ((g & 1u) << 2))) + 8u * (p4 & 1u);
    EPOCH_LOAD(); EPOCH_WAIT();
    BARX();
    STAMP(1);
    bf16x8 xf[2][4], wf[2][4];
    unsigned xa[2], wa[4];
#pragma unroll
    for (int i = 0; i < 4; ++i) { wa[i] = wb[i]; wf[0][i] = ldtr(wa[i], wa[i] + 512u); xf[0][i] = ld128(xb[0] + 2048u * i); }
    xa[0] = xb[0]; xa[1] = xb[1];
    for (unsigned it = 0; it < 16; ++it) {
      const unsigned nso = ((it + 1u) & 3u) * SLOT;
      SB();
#pragma unroll
      for (unsigned s = 0; s < 2; ++s) {
        if (s == 1) {
#pragma unroll
          for (int i = 0; i < 4; ++i) { wa[i] = wb[i] + nso; OPAQUE(wa[i]); }
          xa[0] = xb[0] + nso; OPAQUE(xa[0]);
        }
#pragma unroll
        for (unsigned m = 0; m < 16; ++m) {
          const unsigned i = m >> 2, j = m & 3;
          acc[i][j] = MFMA(wf[s][i], xf[s][j], acc[i][j]);
          if (m < 4) xf[s ^ 1][m] = ld128(xa[s ^ 1] + 2048u * m);
          if (m >= 4 && m < 12 && (m & 1) == 0) { const unsigned n = (m - 4) >> 1; wf[s ^ 1][n] = ldtr(wa[n] + 8192u * (s ^ 1), wa[n] + 8192u * (s ^ 1) + 512u); }
          SB();
        }
        if (s == 0) { xa[1] = xb[1] + nso; OPAQUE(xa[1]); }
      }
      if (it != 14) BARX();
      STAMP(32 + it);
    }
#pragma unroll
    for (unsigned i = 0; i < 4; ++i)
#pragma unroll
      for (unsigned j = 0; j < 4; ++j) {
        const u32x2 w = {cvtpk(acc[i][j][0] * QSCALE, acc[i][j][1] * QSCALE), cvtpk(acc[i][j][2] * QSCALE, acc[i][j][3] * QSCALE)};
        *(LAS u32x2*)(ldsb + 3u * SLOT + ((4u * qh + j) * 4u + 2u * dh + (i >> 1)) * 1024u + (16u * (2u * (i & 1u) + (g >> 1)) + c) * 16u + 8u * (g & 1u)) = w;
      }
  }
  STAMP(2);
  BAR();
  bf16x8 qf[2][4];
#pragma unroll
  for (int s = 0; s < 8; ++s) qf[s >> 2][s & 3] = ld128(ldsb + 3u * SLOT + w4 * 8192u + s * 1024u + l16);
  STAMP(3);

  const __amdgpu_buffer_rsrc_t srdB = __builtin_amdgcn_make_buffer_rsrc((void*)(wo + (size_t)(kq * 1024u + grp * 512u) * 2048u + hl * 256u + w4 * 64u), (short)0, 512 * 2048 - 1984, 0x00020000);
  const unsigned vB = (lane >> 2) * 2048u + ((p4 ^ ((g & 1u) << 1)) * 16u);
  const unsigned prot = ((hl + bl) & 3u) * 8u;
  const unsigned bstage = ldsb + 65536u + wid * 8192u;
  auto dma_b4 = [&](unsigned p0) {
#pragma unroll
    for (unsigned j = 0; j < 4; ++j) dma16<0>(bstage + ((p0 + j) & 7u) * 1024u, vB, srdB, ((prot + p0 + j) & 31u) * 32768u);
  };

  unsigned kb2[2], vb4[4];
#pragma unroll
  for (unsigned s = 0; s < 2; ++s) kb2[s] = ldsb + 2048u * (c >> 3) + 128u * (c & 7u) + 16u * ((4u * s + g) ^ fr(c));
#pragma unroll
  for (unsigned i = 0; i < 4; ++i) vb4[i] = ldsb + 2048u * (g >> 1) + 128u * (4u * (g & 1u) + q4) + 16u * ((2u * i + (p4 >> 1)) ^ ((q4 & 2u) | ((g & 1u) << 2))) + 8u * (p4 & 1u);
  f32x4 o[8][2] = {};
  f32x4 negm[2] = {};
  float mref[2] = {0.f, 0.f}, ls[2][2] = {{0.f, 0.f}, {0.f, 0.f}};
  if (grp == 1) { WAITV(8); BAR(); }
  unsigned jm = grp;
  for (unsigned it = 0; it < 8; ++it) {
    const unsigned j = 2u * it + grp;
    const unsigned jm1 = jm == 2u ? 0u : jm + 1u;
    f32x4 s[4][2];
    {
      unsigned ka[2];
      const unsigned so = kaddr(jm);
#pragma unroll
      for (int i = 0; i < 2; ++i) { ka[i] = kb2[i] + so; OPAQUE(ka[i]); }
      bf16x8 kf[16];
#define KRD(m) kf[m] = ld128(ka[(m) & 1] + 4096u * ((m) >> 2) + 1024u * (((m) >> 1) & 1))
      KRD(0); SB(); KRD(1); SB(); KRD(2); SB(); KRD(3); SB(); KRD(4);
      SB();
      float mx[2];
#pragma unroll
      for (int m = 0; m < 16; ++m) {
        const int kt = m >> 2, ds = m & 3;
#pragma unroll
        for (int qt = 0; qt < 2; ++qt) {
          s[kt][qt] = MFMA(kf[m], qf[qt][ds], ds == 0 ? negm[qt] : s[kt][qt]);
          if (qt == 0 && m + 5 < 16) KRD(m + 5);
          if (kt > 0 && ds >= 2) {
            const int e = 2 * (ds - 2);
            if (kt == 1 && ds == 2) mx[qt] = fmaxf(s[0][qt][0], s[0][qt][1]);
            else mx[qt] = fmaxf(fmaxf(mx[qt], s[kt - 1][qt][e]), s[kt - 1][qt][e + 1]);
          }
          SB();
        }
        if (PACE && ds == 3 && j < 14) __builtin_amdgcn_s_sleep(PACE);
      }
#pragma unroll
      for (int qt = 0; qt < 2; ++qt) { mx[qt] = fmaxf(fmaxf(mx[qt], s[3][qt][0]), s[3][qt][1]); mx[qt] = fmaxf(fmaxf(mx[qt], s[3][qt][2]), s[3][qt][3]); }
      if (it == 0 || __any(fmaxf(mx[0], mx[1]) > THR)) {
#pragma unroll
        for (int qt = 0; qt < 2; ++qt) {
          float dl = red4_max(mx[qt]);
          if (it != 0) {
            dl = fmaxf(dl, 0.f);
            const float f = ex2(-dl);
            ls[qt][0] *= f; ls[qt][1] *= f;
#pragma unroll
            for (int dt = 0; dt < 8; ++dt) o[dt][qt] *= f;
          }
          mref[qt] += dl;
#pragma unroll
          for (int kt = 0; kt < 4; ++kt) s[kt][qt] -= dl;
          negm[qt] = f32x4{-mref[qt], -mref[qt], -mref[qt], -mref[qt]};
        }
      }
    }
    bf16x8 pf[2][2];
#pragma unroll
    for (int qt = 0; qt < 2; ++qt)
#pragma unroll
      for (int e = 0; e < 4; ++e) { s[0][qt][e] = ex2(s[0][qt][e]); s[1][qt][e] = ex2(s[1][qt][e]); }
    SB();
    pf[0][0] = pack8(s[0][0], s[1][0]); pf[0][1] = pack8(s[0][1], s[1][1]);
    OPAQUE(pf[0][0]); OPAQUE(pf[0][1]);
    SB();
#pragma unroll
    for (int e = 0; e < 4; ++e)
#pragma unroll
      for (int qt = 0; qt < 2; ++qt) { ls[qt][0] = fadd_s(ls[qt][0], s[0][qt][e]); ls[qt][1] = fadd_s(ls[qt][1], s[1][qt][e]); }
    SB();
    if (it == 3) STAMP(48); if (it == 7) STAMP(52);
    if (j - 1u < 14u) { dma_v(j + 1u, vaddr(jm1)); WAITV(4); }
    else if (j == 15u) { dma_b4(0); dma_b4(4); }
    if (it == 3) STAMP(49); if (it == 7) STAMP(53);
    BARX();
    STAMP(4 + 2 * it);
    {
      if (j <= 12u) dma_k(j + 3u, kaddr(jm));
      else if (j == 14u) { dma_b4(0); dma_b4(4); }
      if (it == 3) STAMP(50); if (it == 7) STAMP(54);
      unsigned va[4];
      const unsigned so = vaddr(jm);
#pragma unroll
      for (int i = 0; i < 4; ++i) { va[i] = vb4[i] + so; OPAQUE(va[i]); }
      bf16x8 vf[16];
#define VRD(m) vf[m] = ldtr(va[(m) & 3] + 8192u * ((m) >> 3) + 1024u * (((m) >> 2) & 1), va[(m) & 3] + 8192u * ((m) >> 3) + 1024u * (((m) >> 2) & 1) + 4096u)
      VRD(0); SB(); VRD(1); SB(); VRD(2); SB(); VRD(3);
      SB();
#pragma unroll
      for (int m = 0; m < 16; ++m) {
        const int ks = m >> 3, dt = m & 7;
#pragma unroll
        for (int qt = 0; qt < 2; ++qt) {
          o[dt][qt] = MFMA(vf[m], pf[ks][qt], o[dt][qt]);
          if (qt == 0 && m + 4 < 16) VRD(m + 4);
          if (ks == 0) { const int kt = 2 + (dt >> 2), e = dt & 3; s[kt][qt][e] = ex2(s[kt][qt][e]); if (dt == 7) pf[1][qt] = pack8(s[2][qt], s[3][qt]); }
          else { const int kt = 2 + (dt >> 2), e = dt & 3; ls[qt][dt & 1] = fadd_s(ls[qt][dt & 1], s[kt][qt][e]); }
          SB();
        }
        if (PACE && (dt & 3) == 3 && j < 14) __builtin_amdgcn_s_sleep(PACE);
      }
      if (it == 3) STAMP(51); if (it == 7) STAMP(55);
      if (j <= 12u) WAITV(4); else if (j == 13u) WAITV(0); else if (j == 14u) WAITV(8);
    }
    jm = jm == 0u ? 2u : jm - 1u;
    BARX();
    STAMP(5 + 2 * it);
  }

  const __amdgpu_buffer_rsrc_t srdWS = __builtin_amdgcn_make_buffer_rsrc((void*)ws, (short)0, (int)WS_FLAGO, 0x00020000);
  const u64 tag = epoch + 1ull;
  const unsigned xo0 = ldsb + 131072u + w4 * 8192u + l16, xo1 = ldsb + 32768u + w4 * 8192u + l16, xml = ldsb + 16384u + w4 * 1024u + l16;
  const float lt0 = red4_sum(ls[0][0] + ls[0][1]), lt1 = red4_sum(ls[1][0] + ls[1][1]);
  if (grp == 0) {
#pragma unroll
    for (int dt = 0; dt < 4; ++dt)
#pragma unroll
      for (int qt = 0; qt < 2; ++qt) { *(LAS f32x4*)(xo0 + (2 * dt + qt) * 1024u) = o[dt][qt]; *(LAS f32x4*)(xo1 + (2 * dt + qt) * 1024u) = o[4 + dt][qt]; }
    *(LAS f32x4*)xml = f32x4{mref[0], lt0, mref[1], lt1};
    BAR();
  } else {
    const f32x4 ot = *(LAS f32x4*)xml;
    float wa_[2], wb_[2];
    {
      const float M0 = fmaxf(mref[0], ot[0]), M1 = fmaxf(mref[1], ot[2]);
      const float a0 = ex2(mref[0] - M0), b0 = ex2(ot[0] - M0), a1 = ex2(mref[1] - M1), b1 = ex2(ot[2] - M1);
      const float r0 = __builtin_amdgcn_rcpf(a0 * lt0 + b0 * ot[1]), r1 = __builtin_amdgcn_rcpf(a1 * lt1 + b1 * ot[3]);
      wa_[0] = a0 * r0; wb_[0] = b0 * r0; wa_[1] = a1 * r1; wb_[1] = b1 * r1;
    }
#pragma unroll
    for (int s2 = 0; s2 < 4; ++s2)
#pragma unroll
      for (int qt = 0; qt < 2; ++qt) {
        const unsigned xo = s2 < 2 ? xo0 : xo1;
        const f32x4 u = *(LAS f32x4*)(xo + (4 * (s2 & 1) + qt) * 1024u), v = *(LAS f32x4*)(xo + (4 * (s2 & 1) + 2 + qt) * 1024u);
        const bf16x8 f = pack8(o[2 * s2][qt] * wa_[qt] + u * wb_[qt], o[2 * s2 + 1][qt] * wa_[qt] + v * wb_[qt]);
        st16_sc1(__builtin_bit_cast(u32x4, f), l16, srdWS, WS_OFRAG + ((b * 8u + 2u * w4 + qt) * 128u + h * 4u + s2) * 1024u);
      }
  }
  STAMP(20);
  bf16x8 bw[16][2];
  const unsigned bt0 = bstage + 64u * (4u * g + q4) + 32u * (g & 1u) + 8u * p4, bt1 = bt0 ^ 32u;
#pragma unroll
  for (unsigned rd = 0; rd < 8; ++rd) {
    if (grp == 1 && rd < 2) { if (rd == 0) WAITV(12); else WAITV(12); }
    else if (grp == 1 && rd == 2) {
      WAITV(8);
      if (lane == 0) st_flag(ws + WS_FLAGO + blockIdx.x * 128u + w4 * 8u, tag);
      WAITV(5);
    }
    else if (grp == 1 && rd == 3) WAITV(5);
    else if (rd < 7) WAITV(4); else WAITV(0);
#pragma unroll
    for (unsigned u = 0; u < 2; ++u) {
      const unsigned po = ((4u * rd + 2u * u) & 7u) * 1024u;
      bw[2 * rd + u][0] = ldtr(bt0 + po, bt0 + po + 1024u); bw[2 * rd + u][1] = ldtr(bt1 + po, bt1 + po + 1024u);
    }
    if (rd < 6) { WAITL0(); dma_b4(4u * rd + 8u); }
  }
  STAMP(21);
  if (wid == 0) {
    const char* fp = ws + WS_FLAGO + (((blockIdx.x & ~56u) | (((lane >> 2) & 7u) << 3)) * 128u) + p4 * 8u;
    for (unsigned sp = 0; sp < SPIN_CAP; ++sp) {
      if (__all(ld_flag(fp) == tag)) break;
      __builtin_amdgcn_s_sleep(1);
    }
  }
  BAR();
  STAMP(22);
  const unsigned ah = (wid + hl) & 7u;
  auto dma_a1 = [&](unsigned i, unsigned jj) {
    const unsigned rbk = (kq + 1u + i) & 3u;
    dma16<2>(ldsb + (i & 1u) * 65536u + ah * 8192u + jj * 1024u, l16, srdWS, WS_OFRAG + ((b * 8u + 2u * rbk + (jj & 1u)) * 128u + (kq * 8u + ah) * 4u + (jj >> 1)) * 1024u);
  };
#pragma unroll
  for (unsigned jj = 0; jj < 8; ++jj) dma_a1(0, jj);
#pragma unroll
  for (unsigned jj = 0; jj < 8; ++jj) dma_a1(1, jj);
  WAITV(8); BARX();
  STAMP(23);
  const unsigned red = ldsb + 4u * SLOT;
  const unsigned abase = ldsb + grp * 32768u + l16;
  f32x4 prev[2];
#pragma unroll
  for (unsigned i = 0; i < 4; ++i) {
    unsigned aa[4];
#pragma unroll
    for (unsigned cc = 0; cc < 4; ++cc) { aa[cc] = abase + (i & 1u) * 65536u + ((prot + 8u * cc) & 31u) * 1024u; OPAQUE(aa[cc]); }
    bf16x8 af[32];
#define ARD(m) af[m] = ld128(aa[(m) >> 3] + 1024u * ((m) & 7))
    ARD(0); SB(); ARD(1); SB(); ARD(2); SB(); ARD(3); SB();
    f32x4 u0, u1;
    if (i > 0) { u0 = *(LAS f32x4*)(red + ((i - 1u) & 1u) * 16384u + (wid ^ 4u) * 2048u + l16); u1 = *(LAS f32x4*)(red + ((i - 1u) & 1u) * 16384u + (wid ^ 4u) * 2048u + 1024u + l16); }
    SB();
    f32x4 acc[2][2] = {};
#pragma unroll
    for (int m = 0; m < 32; ++m) {
      const int kk = m >> 1, qt = m & 1;
      acc[0][qt] = MFMA(bw[kk][0], af[m], acc[0][qt]);
      if (m + 4 < 32) ARD(m + 4);
      SB();
      acc[1][qt] = MFMA(bw[kk][1], af[m], acc[1][qt]);
      if (i > 0 && m == 2) {
        const unsigned dst = (blockIdx.x & ~6u) | (((kq + i) & 3u) << 1);
        const unsigned so = WS_SLAB + (dst * 4u + kq) * 16384u + wid * 2048u;
        st16_sc1(__builtin_bit_cast(u32x4, prev[0] + u0), l16, srdWS, so); st16_sc1(__builtin_bit_cast(u32x4, prev[1] + u1), l16, srdWS, so + 1024u);
      }
      if (i > 0 && i < 3 && m >= 4 && m < 20 && (m & 1) == 0) dma_a1(i + 1, (m - 4) >> 1);
      if (i > 0 && m == 22) {
        if (i < 3) WAITV(8); else WAITV(0);
        const unsigned dst = (blockIdx.x & ~6u) | (((kq + i) & 3u) << 1);
        if (lane == 0) st_flag(ws + WS_FLAGS + (dst * 4u + kq) * 128u + wid * 8u, tag);
      }
      SB();
    }
    {
      const unsigned rw = red + (i & 1u) * 16384u + wid * 2048u + l16;
      if (grp == 0) { *(LAS f32x4*)(rw) = acc[1][0]; *(LAS f32x4*)(rw + 1024u) = acc[1][1]; prev[0] = acc[0][0]; prev[1] = acc[0][1]; }
      else          { *(LAS f32x4*)(rw) = acc[0][0]; *(LAS f32x4*)(rw + 1024u) = acc[0][1]; prev[0] = acc[1][0]; prev[1] = acc[1][1]; }
    }
    if (i == 0) WAITV(0); else if (i < 3) WAITV(1);
    BAR();
    STAMP(24 + i);
  }
  {
    f32x4 a0 = prev[0] + *(LAS f32x4*)(red + 16384u + (wid ^ 4u) * 2048u + l16), a1 = prev[1] + *(LAS f32x4*)(red + 16384u + (wid ^ 4u) * 2048u + 1024u + l16);
    const unsigned src = (kq + 1u + (lane & 3u) % 3u) & 3u;
    const char* fp = ws + WS_FLAGS + (blockIdx.x * 4u + src) * 128u + wid * 8u;
    for (unsigned sp = 0; sp < SPIN_CAP; ++sp) {
      if (__all(ld_flag(fp) == tag)) break;
      __builtin_amdgcn_s_sleep(1);
    }
    asm volatile("" ::: "memory");
    STAMP(28);
#pragma unroll
    for (unsigned jj = 1; jj < 4; ++jj) {
      const unsigned so = WS_SLAB + (blockIdx.x * 4u + ((kq + jj) & 3u)) * 16384u + wid * 2048u;
      a0 += __builtin_bit_cast(f32x4, __builtin_amdgcn_raw_buffer_load_b128(srdWS, l16, so, 16));
      a1 += __builtin_bit_cast(f32x4, __builtin_amdgcn_raw_buffer_load_b128(srdWS, l16, so + 1024u, 16));
    }
    char* op = out + (size_t)(b * 128u + kq * 32u + c) * 2048u + hl * 256u + w4 * 64u + grp * 32u + g * 8u;
    *(u32x2*)op = u32x2{cvtpk(a0[0], a0[1]), cvtpk(a0[2], a0[3])};
    *(u32x2*)(op + 32768) = u32x2{cvtpk(a1[0], a1[1]), cvtpk(a1[2], a1[3])};
    if (h == 0 && tid == 0) st_flag(ws + WS_EPOCH + b * 128u, tag);
  }
  STAMP(29);
  ((unsigned*)(ws + (32u << 20)))[(blockIdx.x * 8 + wid) * 64 + lane] = tsv;
}

__global__ void probe(const unsigned* t) {
  __shared__ int smin[256], sval[256];
  const unsigned ref = t[0];
  const int b = threadIdx.x;
  smin[b] = (int)(t[b * 512] - ref); sval[b] = (int)(t[b * 512 + 0 * 64 + 29] - ref);
  __syncthreads();
  if (b == 0) {
    int mn = 1 << 30, mx = -(1 << 30); long long sum = 0;
    for (int i = 0; i < 256; ++i) { mn = min(mn, smin[i]); mx = max(mx, sval[i]); sum += sval[i]; }
    long long v = (1 ? mx : (int)(sum / 256)) - mn;
    if (v < 0) v = 0; if (v > 20000) v = 20000;
    const unsigned long long t0 = wall_clock64();
    while (wall_clock64() - t0 < (unsigned long long)(0 * v)) __builtin_amdgcn_s_sleep(4);
  }
}
extern "C" void kernel_launch(void* const* d_in, const int* in_sizes, int n_in, void* d_out, int out_size, void* d_ws, size_t ws_size, hipStream_t stream) {
  (void)in_sizes; (void)n_in; (void)out_size; (void)ws_size;
  (void)hipFuncSetAttribute((const void*)mha_fused, hipFuncAttributeMaxDynamicSharedMemorySize, 163840);
  mha_fused<<<256, 512, 163840, stream>>>((const char*)d_in[0], (const char*)d_in[1], (const char*)d_in[2], (const char*)d_in[3], (const char*)d_in[4], (char*)d_out, (char*)d_ws);
  probe<<<1, 256, 0, stream>>>((const unsigned*)((char*)d_ws + (32u << 20)));
}
```

```cpp
#include <hip/hip_runtime.h>
#include <hip/hip_bf16.h>
#include <type_traits>

typedef __bf16   bf16x8 __attribute__((ext_vector_type(8)));
typedef __bf16   bf16x4 __attribute__((ext_vector_type(4)));
typedef __bf16   bf16x2 __attribute__((ext_vector_type(2)));
typedef float    f32x4  __attribute__((ext_vector_type(4)));
typedef float    f32x2  __attribute__((ext_vector_type(2)));
typedef unsigned u32x4  __attribute__((ext_vector_type(4)));
typedef unsigned u32x2  __attribute__((ext_vector_type(2)));
typedef unsigned long long u64;
#define LAS __attribute__((address_space(3)))
#define GAS __attribute__((address_space(1)))
#define DI __device__ __forceinline__
#define MFMA(a, b, c) __builtin_amdgcn_mfma_f32_16x16x32_bf16((a), (b), (c), 0, 0, 0)
#define WAITV(n) asm volatile("s_waitcnt vmcnt(" #n ")" ::: "memory")
#define WAITL0() asm volatile("s_waitcnt lgkmcnt(0)" ::: "memory")
#define SB() __builtin_amdgcn_sched_barrier(0)
#define OPAQUE(x) asm volatile("" : "+v"(x))
#define BAR() do { asm volatile("s_waitcnt lgkmcnt(0)" ::: "memory"); __builtin_amdgcn_s_barrier(); asm volatile("" ::: "memory"); } while (0)
#define BARX() do { asm volatile("" ::: "memory"); __builtin_amdgcn_s_barrier(); asm volatile("" ::: "memory"); } while (0)

constexpr int SQ = 128, DM = 1024, H_ = 32, SK = 1024, DH = 128;
constexpr unsigned SLOT = 32768u;
constexpr float QSCALE = 0.12751743082f;
constexpr float ROFF = 7.0f;
constexpr unsigned WS_OFRAG = 0u;
constexpr unsigned WS_SLAB  = 8u << 20;
constexpr unsigned WS_FLAGO = 32u << 20;
constexpr unsigned WS_EPOCH = WS_FLAGO + 32768u;
constexpr unsigned SPIN_CAP = 1u << 20;

DI unsigned rfl(unsigned x) { return __builtin_amdgcn_readfirstlane(x); }
DI unsigned cvtpk(float lo, float hi) { f32x2 v = {lo, hi}; bf16x2 b = __builtin_convertvector(v, bf16x2); return __builtin_bit_cast(unsigned, b); }
DI bf16x8 pack8(const f32x4& x, const f32x4& y) {
  u32x4 w = {cvtpk(x[0], x[1]), cvtpk(x[2], x[3]), cvtpk(y[0], y[1]), cvtpk(y[2], y[3])};
  return __builtin_bit_cast(bf16x8, w);
}
DI float ex2(float x) { return __builtin_amdgcn_exp2f(x); }
DI float red4_max(float x) {
  auto a = __builtin_amdgcn_permlane32_swap(__float_as_uint(x), __float_as_uint(x), false, false);
  x = fmaxf(__uint_as_float(a[0]), __uint_as_float(a[1]));
  auto b = __builtin_amdgcn_permlane16_swap(__float_as_uint(x), __float_as_uint(x), false, false);
  return fmaxf(__uint_as_float(b[0]), __uint_as_float(b[1]));
}
template <int POL> DI void dma16(unsigned m0v, unsigned voff, __amdgpu_buffer_rsrc_t srd, unsigned soff) {
  if constexpr (POL == 1)      asm volatile("s_mov_b32 m0, %0\n\ts_nop 0\n\tbuffer_load_dwordx4 %1, %2, %3 offen nt lds" :: "s"(m0v), "v"(voff), "s"(srd), "s"(soff) : "memory");
  else if constexpr (POL == 2) asm volatile("s_mov_b32 m0, %0\n\ts_nop 0\n\tbuffer_load_dwordx4 %1, %2, %3 offen sc1 lds" :: "s"(m0v), "v"(voff), "s"(srd), "s"(soff) : "memory");
  else                         asm volatile("s_mov_b32 m0, %0\n\ts_nop 0\n\tbuffer_load_dwordx4 %1, %2, %3 offen lds" :: "s"(m0v), "v"(voff), "s"(srd), "s"(soff) : "memory");
}
DI void st16_sc1(u32x4 v, unsigned voff, __amdgpu_buffer_rsrc_t srd, unsigned soff) {
  asm volatile("buffer_store_dwordx4 %0, %1, %2, %3 offen sc1\n\ts_nop 1" :: "v"(v), "v"(voff), "s"(srd), "s"(soff) : "memory");
}
DI bf16x8 ld128(unsigned addr) { return *(LAS bf16x8*)addr; }
DI bf16x8 ldtr(unsigned a0, unsigned a1) {
  bf16x4 lo = __builtin_amdgcn_ds_read_tr16_b64_v4bf16((LAS bf16x4*)a0);
  bf16x4 hi = __builtin_amdgcn_ds_read_tr16_b64_v4bf16((LAS bf16x4*)a1);
  return __builtin_shufflevector(lo, hi, 0, 1, 2, 3, 4, 5, 6, 7);
}
DI unsigned fr(unsigned row) { return ((row >> 1) & 7u) ^ (((row >> 2) ^ (row >> 3)) & 1u); }
DI u64 ld_flag(const char* p) { return __hip_atomic_load((GAS u64*)p, __ATOMIC_RELAXED, __HIP_MEMORY_SCOPE_AGENT); }
DI void st_flag(char* p, u64 v) { __hip_atomic_store((GAS u64*)p, v, __ATOMIC_RELAXED, __HIP_MEMORY_SCOPE_AGENT); }
struct Sched { u64 ka, va, wait[2]; unsigned kgo, vgo; };
constexpr int PRE[5] = {-1, 21, 24, 27, 29};
constexpr Sched make_sched() {
  Sched s = {0, 0, {0, 0}, 0, 0};
  for (int t = 0; t < 16; ++t) {
    int k = t % 3 == 2 ? 1 : 8 + t % 3, v = t % 3 == 0 ? 0 : 1 + t % 3;
    for (int i = 1; i < 5; ++i) { if (PRE[i] == t) k = 3 + i; if (PRE[i] == 16 + t) v = 3 + i; }
    s.ka |= (u64)k << (4 * t); s.va |= (u64)v << (4 * t);
  }
  int seq[2][40] = {}, n[2] = {0, 0};
  const int first[3] = {16, 2, 17};
  for (int i = 0; i < 3; ++i) seq[1][n[1]++] = first[i];
  for (int i = 0; i < 5; ++i) if (PRE[i] >= 0) seq[1][n[1]++] = PRE[i];
  for (int j = 0; j < 16; ++j) {
    bool pk = j + 2 > 15 || j == 0, pv = j + 1 > 15 || j == 0;
    for (int i = 0; i < 5; ++i) { pk = pk || PRE[i] == j + 2; pv = pv || PRE[i] == 17 + j; }
    if (!pk) { seq[(j + 1) & 1][n[(j + 1) & 1]++] = j + 2; s.kgo |= 1u << (j - 1); }
    if (!pv) { seq[j & 1][n[j & 1]++] = 17 + j; s.vgo |= 1u << j; }
    for (int g = 0; g < 2; ++g) {
      int w = 15;
      for (int i = 0; i < n[g]; ++i) if ((j < 15 && seq[g][i] == j + 1) || seq[g][i] == 16 + j) w = n[g] - 1 - i < 7 ? n[g] - 1 - i : 7;
      s.wait[g] |= (u64)w << (4 * j);
    }
  }
  return s;
}
constexpr Sched SCH = make_sched();
DI unsigned kaddr(unsigned j) { return ((unsigned)(SCH.ka >> (4u * j)) & 15u) * 16384u; }
DI unsigned vaddr(unsigned j) { return ((unsigned)(SCH.va >> (4u * j)) & 15u) * 16384u; }

__global__ __launch_bounds__(512, 2)
void mha_fused(const char* __restrict__ x, const char* __restrict__ kc, const char* __restrict__ vc, const char* __restrict__ wq,
               const char* __restrict__ wo, char* __restrict__ out, char* ws) {
  extern __shared__ __attribute__((aligned(16))) char lds[];
  const unsigned tid = threadIdx.x, lane = tid & 63u, wid = rfl(tid >> 6), grp = wid >> 2, w4 = wid & 3u;
  const unsigned c = lane & 15u, g = lane >> 4, q4 = c >> 2, p4 = lane & 3u, l16 = lane * 16u;
  const unsigned bid = blockIdx.x;
  const unsigned xg = bid & 7u, idx = bid >> 3, bl = idx >> 3, hl = idx & 7u, kq = xg >> 1;
  const unsigned b = 4u * (xg & 1u) + bl, h = 8u * kq + hl, unit = b * 32u + h;
  const unsigned ldsb = rfl((unsigned)(size_t)(LAS char*)lds);
  const unsigned rl = lane >> 3, ckr = ((lane & 7u) ^ fr(rl)) * 16u;
  const __amdgpu_buffer_rsrc_t srdX = __builtin_amdgcn_make_buffer_rsrc((void*)(x + (size_t)b * (SQ * DM * 2)), (short)0, SQ * DM * 2, 0x00020000);
  const __amdgpu_buffer_rsrc_t srdW = __builtin_amdgcn_make_buffer_rsrc((void*)(wq + h * 256u), (short)0, (int)(DM * H_ * DH * 2 - h * 256u), 0x00020000);
  const unsigned k0 = (4u * (hl >> 1) + 8u * (bl >> 1) + 2u * (bl & 1u) + xg) & 15u;
  const unsigned ckw = ((lane & 7u) ^ (rl & 2u)) * 16u;
  const unsigned vx0 = rl * 2048u + ckr, vx1 = rl * 2048u + (ckr ^ 80u), vw0 = rl * 8192u + ckw, vw1 = rl * 8192u + (ckw ^ 64u);
  auto dma_q = [&](unsigned i) {
    const unsigned kt = (k0 + i) & 15u;
    const unsigned m = ldsb + (i & 3u) * SLOT + w4 * 4096u, sx = w4 * 32u * 2048u + kt * 128u, sw = (kt * 64u + w4 * 16u) * 8192u;
    dma16<0>(m, vx0, srdX, sx);                    dma16<0>(m + 1024u, vx1, srdX, sx + 16384u);
    dma16<0>(m + 2048u, vx0, srdX, sx + 32768u);   dma16<0>(m + 3072u, vx1, srdX, sx + 49152u);
    dma16<0>(m + 16384u, vw0, srdW, sw);           dma16<0>(m + 17408u, vw0, srdW, sw + 128u);
    dma16<0>(m + 18432u, vw1, srdW, sw + 65536u);  dma16<0>(m + 19456u, vw1, srdW, sw + 65664u);
  };
  if (grp == 1) {
#pragma nounroll
    for (unsigned i = 0; i < 2; ++i) dma_q(i);
  }
  const __amdgpu_buffer_rsrc_t srdK = __builtin_amdgcn_make_buffer_rsrc((void*)(kc + (size_t)unit * (SK * DH * 2)), (short)0, SK * DH * 2, 0x00020000);
  const __amdgpu_buffer_rsrc_t srdV = __builtin_amdgcn_make_buffer_rsrc((void*)(vc + (size_t)unit * (SK * DH * 2)), (short)0, SK * DH * 2, 0x00020000);
  const unsigned vk0 = rl * 256u + ckr, vk1 = rl * 256u + (ckr ^ 80u), vv = rl * 256u + (((lane & 7u) ^ (rl & 6u)) * 16u);
  auto dma_k = [&](unsigned t, unsigned la) {
    const unsigned m = ldsb + la + w4 * 4096u, so = t * 16384u + w4 * 4096u;
    dma16<1>(m, vk0, srdK, so);                  dma16<1>(m + 1024u, vk0, srdK, so + 128u);
    dma16<1>(m + 2048u, vk1, srdK, so + 2048u);  dma16<1>(m + 3072u, vk1, srdK, so + 2176u);
  };
  auto dma_v = [&](unsigned t, unsigned la) {
    const unsigned m = ldsb + la + w4 * 4096u, so = t * 16384u + w4 * 4096u;
    dma16<1>(m, vv, srdV, so);                  dma16<1>(m + 1024u, vv, srdV, so + 128u);
    dma16<1>(m + 2048u, vv, srdV, so + 2048u);  dma16<1>(m + 3072u, vv, srdV, so + 2176u);
  };

  auto pre = [&](int i) { if (PRE[i] >= 16) dma_v(PRE[i] - 16, vaddr(PRE[i] - 16)); else if (PRE[i] >= 0) dma_k(PRE[i], kaddr(PRE[i])); };
  const u64 wtab = grp ? SCH.wait[1] : SCH.wait[0];
  auto waitn = [&](unsigned n) {
    const unsigned w = (unsigned)(wtab >> (4u * n)) & 15u;
    if (w == 1u) WAITV(4); else if (w == 0u) WAITV(0); else if (w == 2u) WAITV(8); else if (w == 3u) WAITV(12); else if (w == 4u) WAITV(16);
    else if (w == 5u) WAITV(20); else if (w == 6u) WAITV(24); else if (w != 15u) WAITV(28);
  };

  u64 epoch;
  const char* epoch_p = ws + WS_EPOCH + b * 128u;
#define EPOCH_LOAD() asm volatile("s_load_dwordx2 %0, %1, 0x0" : "=s"(epoch) : "s"(epoch_p) : "memory")
#define EPOCH_WAIT() asm volatile("s_waitcnt lgkmcnt(0)" : "+s"(epoch) :: "memory")
  u64 pc0; unsigned clen;
  asm volatile("s_getpc_b64 %0\n.Lmha_pc:\n\ts_mov_b32 %1, .Lmha_end-.Lmha_pc" : "=s"(pc0), "=s"(clen));
  const __amdgpu_buffer_rsrc_t srdC = __builtin_amdgcn_make_buffer_rsrc((void*)pc0, (short)0, (int)clen, 0x00020000);
  auto code_touch = [&](unsigned la) {
    asm volatile("s_mov_b32 m0, %0\n\ts_nop 0\n\tbuffer_load_dword %1, %2, 0 offen lds" :: "s"(la), "v"((lane * 32u + idx) * 64u), "s"(srdC) : "memory"); };
  if (grp == 1) {
    const unsigned kve = 3u + ((hl + xg) & 7u);
    WAITV(8); BARX();
#pragma nounroll
    for (unsigned i = 0; i < 2u; ++i) {
      dma_q(i + 2);
      WAITV(8); BARX();
    }
    dma_q(4);
    for (unsigned i = 2; i < 13; ++i) {
      dma_q(i + 3);
      if (i == kve) { dma_k(0, kaddr(0)); dma_k(1, kaddr(1)); }
      if (i - kve < 3u) WAITV(24); else WAITV(16);
      BARX();
    }
    dma_v(0, vaddr(0)); dma_k(2, kaddr(2));
    WAITV(16); BARX();
    dma_v(1, vaddr(1));
    WAITV(12); BARX();
    pre(0); pre(1);
    BARX();
    EPOCH_LOAD(); EPOCH_WAIT();
    pre(2);
  } else {
    const unsigned dh = w4 & 1u, qh = w4 >> 1;
    __builtin_amdgcn_s_setprio(1);
    f32x4 acc[4][4] = {};
    unsigned xb[2], wb[4];
#pragma unroll
    for (unsigned s = 0; s < 2; ++s) xb[s] = ldsb + 8192u * qh + 1024u * (c >> 3) + 128u * (c & 7u) + 16u * ((4u * s + g) ^ fr(c));
#pragma unroll
    for (unsigned i = 0; i < 4; ++i) wb[i] = ldsb + 16384u + 2048u * g + 1024u * dh + 128u * q4 + 16u * ((2u * i + (p4 >> 1)) ^ ((q4 & 2u) | ((g & 1u) << 2))) + 8u * (p4 & 1u);
    if (wid == 0) code_touch(ldsb + 131072u);
    EPOCH_LOAD(); EPOCH_WAIT();
    BARX();
    bf16x8 xf[2][4], wf[2][4];
    unsigned xa[2], wa[4];
#pragma unroll
    for (int i = 0; i < 4; ++i) { wa[i] = wb[i]; wf[0][i] = ldtr(wa[i], wa[i] + 512u); xf[0][i] = ld128(xb[0] + 2048u * i); }
    xa[0] = xb[0]; xa[1] = xb[1];
    for (unsigned it = 0; it < 16; ++it) {
      const unsigned nso = ((it + 1u) & 3u) * SLOT;
      SB();
#pragma unroll
      for (unsigned s = 0; s < 2; ++s) {
        if (s == 1) {
#pragma unroll
          for (int i = 0; i < 4; ++i) { wa[i] = wb[i] + nso; OPAQUE(wa[i]); }
          xa[0] = xb[0] + nso; OPAQUE(xa[0]);
        }
#pragma unroll
        for (unsigned m = 0; m < 16; ++m) {
          const unsigned i = m >> 2, j = m & 3;
          acc[i][j] = MFMA(wf[s][i], xf[s][j], acc[i][j]);
          if (m < 4) xf[s ^ 1][m] = ld128(xa[s ^ 1] + 2048u * m);
          if (m >= 4 && m < 12 && (m & 1) == 0) { const unsigned n = (m - 4) >> 1; wf[s ^ 1][n] = ldtr(wa[n] + 8192u * (s ^ 1), wa[n] + 8192u * (s ^ 1) + 512u); }
          SB();
        }
        if (s == 0) { xa[1] = xb[1] + nso; OPAQUE(xa[1]); WAITV(0); BAR(); }
      }
    }
#pragma unroll
    for (unsigned i = 0; i < 4; ++i)
#pragma unroll
      for (unsigned j = 0; j < 4; ++j) {
        const u32x2 w = {cvtpk(acc[i][j][0] * QSCALE, acc[i][j][1] * QSCALE), cvtpk(acc[i][j][2] * QSCALE, acc[i][j][3] * QSCALE)};
        *(LAS u32x2*)(ldsb + 3u * SLOT + ((4u * qh + j) * 4u + 2u * dh + (i >> 1)) * 1024u + (16u * (2u * (i & 1u) + (g >> 1)) + c) * 16u + 8u * (g & 1u)) = w;
      }
  }
  BAR();
  bf16x8 qf[2][4];
#pragma unroll
  for (int s = 0; s < 8; ++s) qf[s >> 2][s & 3] = ld128(ldsb + 3u * SLOT + w4 * 8192u + s * 1024u + l16);

  const unsigned nl = (hl + 1u) & 7u, blp = 2u * bl;
  const __amdgpu_buffer_rsrc_t srdB = __builtin_amdgcn_make_buffer_rsrc((void*)(wo + (size_t)(kq * 1024u) * 2048u + nl * 256u + w4 * 64u), (short)0, 1024 * 2048 - 1984, 0x00020000);
  const unsigned vB = (lane >> 2) * 2048u + ((p4 ^ ((g & 1u) << 1)) * 16u);
  const unsigned hperm = (unsigned)(0x76543217654321ull >> (4u * blp)) << 4;
  auto hseq = [&](unsigned i) { return (hl + (hperm >> (4u * i))) & 7u; };
  auto bstg = [&](unsigned i, unsigned kh) {
    return ldsb + w4 * 4096u + ((i & 6u) == 4u ? 65536u + (i & 1u) * 32768u + kh * 16384u : (i & 1u) ? 49152u + kh * 81920u : 16384u + kh * 16384u); };
  auto dma_b = [&](unsigned i, unsigned kh) {
    const unsigned so = (hseq(i) * 128u + kh * 64u) * 2048u, la = rfl(bstg(i, kh));
#pragma unroll
    for (unsigned p = 0; p < 4; ++p) dma16<0>(la + p * 1024u, vB, srdB, so + p * 32768u);
  };

  unsigned kb2[2], vb4[4];
#pragma unroll
  for (unsigned s = 0; s < 2; ++s) kb2[s] = ldsb + 2048u * (c >> 3) + 128u * (c & 7u) + 16u * ((4u * s + g) ^ fr(c));
#pragma unroll
  for (unsigned i = 0; i < 4; ++i) vb4[i] = ldsb + 2048u * (g >> 1) + 128u * (4u * (g & 1u) + q4) + 16u * ((2u * i + (p4 >> 1)) ^ ((q4 & 2u) | ((g & 1u) << 2))) + 8u * (p4 & 1u);
  f32x4 o[8][2] = {};
  f32x4 negm[2] = {};
  float mref[2] = {0.f, 0.f};
  f32x4 la[2] = {};
  const bf16x8 ones = __builtin_bit_cast(bf16x8, u32x4{0x3f803f80u, 0x3f803f80u, 0x3f803f80u, 0x3f803f80u});
  BAR();
  if (grp == 1) { pre(3); pre(4); waitn(0u); BARX(); }
  for (unsigned it = 0; it < 8; ++it) {
    const unsigned j = 2u * it + grp;
    f32x4 s[4][2];
    bf16x8 pf[2][2];
    if (it == 7u) __builtin_amdgcn_s_setprio(1);
    {
      unsigned ka[2];
      const unsigned so = kaddr(j);
#pragma unroll
      for (int i = 0; i < 2; ++i) { ka[i] = kb2[i] + so; OPAQUE(ka[i]); }
      bf16x8 kf[16];
#define KRD(m) kf[m] = ld128(ka[((m) >> 1) & 1] + 4096u * (2 * ((m) >> 3) + ((m) & 1)) + 1024u * (((m) >> 2) & 1))
      KRD(0); SB(); KRD(1); SB(); KRD(2); SB(); KRD(3); SB(); KRD(4);
      SB();
      f32x4 p0[2], p1[2];
#pragma unroll
      for (int m = 0; m < 16; ++m) {
        const int kt = 2 * (m >> 3) + (m & 1), ds = (m >> 1) & 3;
#pragma unroll
        for (int qt = 0; qt < 2; ++qt) {
          s[kt][qt] = MFMA(kf[m], qf[qt][ds], ds == 0 ? negm[qt] : s[kt][qt]);
          if (qt == 0 && m + 5 < 16) KRD(m + 5);
          if (m >= 11 && m < 15) { p0[qt][m - 11] = ex2(s[0][qt][m - 11]); p1[qt][m - 11] = ex2(s[1][qt][m - 11]); }
          if (m == 15) { pf[0][qt] = pack8(p0[qt], p1[qt]); OPAQUE(pf[0][qt]); }
          SB();
        }
      }
#pragma unroll
      for (int qt = 0; qt < 2; ++qt) {
#pragma unroll
        for (int e = 0; e < 4; ++e) { p0[qt][e] = ex2(s[2][qt][e]); p1[qt][e] = ex2(s[3][qt][e]); }
        pf[1][qt] = pack8(p0[qt], p1[qt]);
      }
      const u32x4 w0 = __builtin_bit_cast(u32x4, pf[0][0]) | __builtin_bit_cast(u32x4, pf[0][1]) | __builtin_bit_cast(u32x4, pf[1][0]) | __builtin_bit_cast(u32x4, pf[1][1]);
      if (it == 0 || __any(((w0[0] | w0[1] | w0[2] | w0[3]) & 0x40004000u) != 0u)) {
#pragma unroll
        for (int qt = 0; qt < 2; ++qt) {
          float mx = fmaxf(fmaxf(s[0][qt][0], s[0][qt][1]), fmaxf(s[0][qt][2], s[0][qt][3]));
#pragma unroll
          for (int kt = 1; kt < 4; ++kt) mx = fmaxf(fmaxf(mx, fmaxf(s[kt][qt][0], s[kt][qt][1])), fmaxf(s[kt][qt][2], s[kt][qt][3]));
          float dl = red4_max(mx) + ROFF;
          if (it != 0) {
            dl = fmaxf(dl, 0.f);
            const float f = ex2(-dl);
            la[qt] *= f;
#pragma unroll
            for (int dt = 0; dt < 8; ++dt) o[dt][qt] *= f;
          }
          mref[qt] += dl;
          negm[qt] = f32x4{-mref[qt], -mref[qt], -mref[qt], -mref[qt]};
#pragma unroll
          for (int ks = 0; ks < 2; ++ks) {
            f32x4 a, b2;
#pragma unroll
            for (int e = 0; e < 4; ++e) { a[e] = ex2(s[2 * ks][qt][e] - dl); b2[e] = ex2(s[2 * ks + 1][qt][e] - dl); }
            pf[ks][qt] = pack8(a, b2);
          }
        }
      }
    }
    SB();
    if (it != 7u) __builtin_amdgcn_s_setprio(0);
    if (SCH.vgo >> j & 1u) dma_v(j + 1u, vaddr(j + 1u));
    waitn(j);
    BARX();
    {
      if (j < 14u) __builtin_amdgcn_s_sleep(2);
      if (j <= 12u) { if (SCH.kgo >> j & 1u) dma_k(j + 3u, kaddr(j + 3u)); }
      else if (j == 14u) { dma_b(0, 0); dma_b(0, 1); }
      else if (j == 13u && wid == 4u) code_touch(ldsb + 147456u + 2048u);
      unsigned va[4];
      const unsigned so = vaddr(j);
#pragma unroll
      for (int i = 0; i < 4; ++i) { va[i] = vb4[i] + so; OPAQUE(va[i]); }
      bf16x8 vf[16];
#define VRD(m) vf[m] = ldtr(va[(m) & 3] + 8192u * ((m) >> 3) + 1024u * (((m) >> 2) & 1), va[(m) & 3] + 8192u * ((m) >> 3) + 1024u * (((m) >> 2) & 1) + 4096u)
      VRD(0); SB(); VRD(1); SB(); VRD(2); SB(); VRD(3);
      SB();
      la[0] = MFMA(ones, pf[0][0], la[0]); la[1] = MFMA(ones, pf[0][1], la[1]); la[0] = MFMA(ones, pf[1][0], la[0]); la[1] = MFMA(ones, pf[1][1], la[1]);
      SB();
#pragma unroll
      for (int m = 0; m < 16; ++m) {
        const int ks = m >> 3, dt = m & 7;
#pragma unroll
        for (int qt = 0; qt < 2; ++qt) {
          o[dt][qt] = MFMA(vf[m], pf[ks][qt], o[dt][qt]);
          if (qt == 0 && m + 4 < 16) VRD(m + 4);
          SB();
        }
      }
      if (j <= 13u) waitn(j + 1u);
    }
    if (it != 7u) BARX();
  }

  const __amdgpu_buffer_rsrc_t srdWS = __builtin_amdgcn_make_buffer_rsrc((void*)ws, (short)0, (int)WS_FLAGO, 0x00020000);
  const u64 tag = epoch * 6364136223846793005ull + 1442695040888963407ull;
  const unsigned io = (w4 - kq - 1u) & 3u;
  const unsigned xo = ldsb + 65536u + w4 * 16384u;
  const unsigned xml = ldsb + 147456u + io * 4096u + l16;
  const float lt0 = la[0][0], lt1 = la[1][0];
  bf16x8 bw[8][2][2];
  const unsigned bt = 64u * (4u * g + q4) + 32u * ((g ^ grp) & 1u) + 8u * p4;
#define BRD(i) do { const unsigned a_ = bstg(i, grp) + bt, b_ = bstg(i, grp) + (bt ^ 32u); \
    bw[i][0][0] = ldtr(a_, a_ + 1024u); bw[i][0][1] = ldtr(b_, b_ + 1024u); bw[i][1][0] = ldtr(a_ + 2048u, a_ + 3072u); bw[i][1][1] = ldtr(b_ + 2048u, b_ + 3072u); } while (0)
  auto dma_a1 = [&](unsigned r, unsigned i) {
    dma16<2>(ldsb + (r & 1u) * 57344u + (w4 >> 1) * 28672u + (i - 1u) * 4096u + (grp * 2u + (w4 & 1u)) * 1024u, l16, srdWS,
             WS_OFRAG + ((b * 8u + 2u * ((kq + 1u + r) & 3u) + grp) * 128u + (kq * 8u + hseq(i)) * 4u + w4) * 1024u);
  };
  if (grp == 0) {
#pragma unroll
    for (int i = 0; i < 8; ++i) *(LAS f32x4*)(xo + l16 + i * 1024u) = o[i >> 1][i & 1];
    WAITV(8);
    BARX();
    __builtin_amdgcn_s_setprio(0);
    dma_b(1, 0); dma_b(1, 1);
#pragma unroll
    for (int i = 8; i < 16; ++i) *(LAS f32x4*)(xo + l16 + i * 1024u) = o[i >> 1][i & 1];
    *(LAS f32x4*)xml = f32x4{mref[0], lt0, mref[1], lt1};
    BAR();
    WAITV(8); BRD(0); WAITV(0); BRD(1);
    BAR();
    dma_b(2, 0);
  } else {
    BARX();
    const f32x4 ot = *(LAS f32x4*)xml;
    f32x4 xu[16];
#pragma unroll
    for (int i = 0; i < 16; ++i) xu[i] = *(LAS f32x4*)(xo + l16 + i * 1024u);
    float wa_[2], wb_[2];
    {
      const float M0 = fmaxf(mref[0], ot[0]), M1 = fmaxf(mref[1], ot[2]);
      const float a0 = ex2(mref[0] - M0), b0 = ex2(ot[0] - M0), a1 = ex2(mref[1] - M1), b1 = ex2(ot[2] - M1);
      const float r0 = __builtin_amdgcn_rcpf(a0 * lt0 + b0 * ot[1]), r1 = __builtin_amdgcn_rcpf(a1 * lt1 + b1 * ot[3]);
      wa_[0] = a0 * r0; wb_[0] = b0 * r0; wa_[1] = a1 * r1; wb_[1] = b1 * r1;
    }
    bf16x8 of[4][2];
#pragma unroll
    for (int s2 = 0; s2 < 4; ++s2)
#pragma unroll
      for (int qt = 0; qt < 2; ++qt) {
        of[s2][qt] = pack8(o[2 * s2][qt] * wa_[qt] + xu[4 * s2 + qt] * wb_[qt], o[2 * s2 + 1][qt] * wa_[qt] + xu[4 * s2 + 2 + qt] * wb_[qt]);
        st16_sc1(__builtin_bit_cast(u32x4, of[s2][qt]), l16, srdWS, WS_OFRAG + ((b * 8u + 2u * w4 + qt) * 128u + h * 4u + s2) * 1024u);
      }
#pragma unroll
    for (int s2 = 0; s2 < 4; ++s2)
#pragma unroll
      for (int qt = 0; qt < 2; ++qt) *(LAS bf16x8*)(ldsb + (s2 >> 1) * 147456u + ((io * 2u + qt) * 2u + (s2 & 1)) * 1024u + l16) = of[s2][qt];
    BAR();
    BRD(0); BRD(1); WAITL0();
    dma_b(2, 1);
  }
  f32x4 acc[4][2][2];
  const f32x4 zero4 = {0.f, 0.f, 0.f, 0.f};
  {
    unsigned aa = ldsb + grp * 147456u + l16; OPAQUE(aa);
    bf16x8 af[16];
#define ARD(m) af[m] = ld128(aa + 2048u * ((m) & 7) + 1024u * ((m) >> 3))
    ARD(0); SB(); ARD(1); SB(); ARD(2); SB(); ARD(3); SB();
#pragma unroll
    for (int m = 0; m < 16; ++m) {
      const int sp = m >> 3, ri = (m & 7) >> 1, qt = m & 1;
      acc[ri][qt][0] = MFMA(bw[0][sp][0], af[m], sp ? acc[ri][qt][0] : zero4);
      if (m + 4 < 16) ARD(m + 4);
      SB();
      acc[ri][qt][1] = MFMA(bw[0][sp][1], af[m], sp ? acc[ri][qt][1] : zero4);
      if (m == 3 && grp == 1) {
        WAITV(4);
        if (lane == 0) st_flag(ws + WS_FLAGO + bid * 128u + w4 * 8u, tag);
        __builtin_amdgcn_s_setprio(0);
      }
      if (m == 7) dma_b(3, grp);
      if (m == 11) dma_b(4, grp);
      SB();
    }
    dma_b(5, grp);
    WAITV(12); BRD(2); WAITL0(); dma_b(6, grp);
    WAITV(12); BRD(3); WAITL0(); dma_b(7, grp);
    WAITV(12); BRD(4);
    WAITV(8); BRD(5);
    const char* fp = ws + WS_FLAGO + (((bid & ~56u) | (((lane >> 2) & 7u) << 3)) * 128u) + p4 * 8u;
    if (wid == 0) {
      for (unsigned sp = 0; sp < SPIN_CAP; ++sp) {
        if (__all(ld_flag(fp) == tag)) break;
        __builtin_amdgcn_s_sleep(2);
      }
    }
    WAITV(0); BRD(6); BRD(7);
    BAR();
#pragma unroll
    for (unsigned i = 0; i < 14; ++i) dma_a1(i / 7u, 1u + i % 7u);
    WAITV(11);
    BARX();
  }
  const unsigned t32 = (unsigned)tag;
  const unsigned red = ldsb + 114688u;
  u32x4 sl[3][2];
#define SLD(j0, j1) do { _Pragma("unroll") for (unsigned jj = j0; jj < j1; ++jj) sl[jj >> 1][jj & 1] = __builtin_amdgcn_raw_buffer_load_b128(srdWS, l16, \
      WS_SLAB + ((bid * 4u + ((kq + 1u + (jj >> 1)) & 3u)) * 8u + wid) * 2048u + (jj & 1u) * 1024u, 16); } while (0)
  auto pack24 = [&](const f32x4& v) {
    const unsigned d = __float_as_uint(v[3]);
    return u32x4{__builtin_amdgcn_perm(__float_as_uint(v[0]), d, 0x07060503u), __builtin_amdgcn_perm(__float_as_uint(v[1]), d, 0x07060502u),
                 __builtin_amdgcn_perm(__float_as_uint(v[2]), d, 0x07060501u), t32};
  };
  auto unpack24 = [&](const u32x4& w) {
    const unsigned d = __builtin_amdgcn_perm(__builtin_amdgcn_perm(w[0], w[1], 0x04000c0cu), w[2], 0x0706000cu);
    return f32x4{__uint_as_float(w[0] & 0xffffff00u), __uint_as_float(w[1] & 0xffffff00u), __uint_as_float(w[2] & 0xffffff00u), __uint_as_float(d)};
  };
  f32x4 prev[2];
  auto rowblk = [&](auto R) __attribute__((always_inline)) {
    constexpr unsigned r = decltype(R)::value;
    unsigned aa = ldsb + (r & 1u) * 57344u + grp * 28672u + l16; OPAQUE(aa);
    bf16x8 af[28];
#define ORD(m) af[m] = ld128(aa + 4096u * ((m) >> 2) + 2048u * ((m) & 1) + 1024u * (((m) >> 1) & 1))
    ORD(0); SB(); ORD(1); SB(); ORD(2); SB(); ORD(3); SB();
    f32x4 pv0, pv1;
    if (r > 0) { pv0 = *(LAS f32x4*)(red + ((r - 1u) & 1u) * 16384u + (wid ^ 4u) * 2048u + l16); pv1 = *(LAS f32x4*)(red + ((r - 1u) & 1u) * 16384u + (wid ^ 4u) * 2048u + 1024u + l16); }
    SB();
    f32x4 ac2[2][2];
#pragma unroll
    for (int m = 0; m < 28; ++m) {
      const int hi = 1 + (m >> 2), sp = (m >> 1) & 1, qt = m & 1;
      if (r == 0 && m == 8) { WAITV(7); BARX(); }
      if (sp) ac2[qt][0] = MFMA(bw[hi][sp][0], af[m], m < 4 ? zero4 : ac2[qt][0]); else acc[r][qt][0] = MFMA(bw[hi][sp][0], af[m], acc[r][qt][0]);
      if (m + 4 < 28) ORD(m + 4);
      SB();
      if (sp) ac2[qt][1] = MFMA(bw[hi][sp][1], af[m], m < 4 ? zero4 : ac2[qt][1]); else acc[r][qt][1] = MFMA(bw[hi][sp][1], af[m], acc[r][qt][1]);
      if ((r == 1 || r == 2) && m < 14 && (m & 1) == 0) dma_a1(r + 1u, 1u + (m >> 1));
      if (r > 0 && (m == (r == 3 ? 2 : 14) || m == (r == 3 ? 5 : 17))) {
        const unsigned dst = (bid & ~6u) | (((kq + r) & 3u) << 1);
        const unsigned so = WS_SLAB + ((dst * 4u + kq) * 8u + wid) * 2048u;
        if (m == (r == 3 ? 2 : 14)) st16_sc1(pack24(prev[0] + pv0), l16, srdWS, so); else st16_sc1(pack24(prev[1] + pv1), l16, srdWS, so + 1024u);
      }
      if (r == 3 && m >= 12 && m < 20 && (m & 1) == 0) SLD(5 - ((m - 12) >> 1), 6 - ((m - 12) >> 1));
      if (r == 3 && m == 26) SLD(0, 2);
      SB();
    }
    const unsigned rw = red + (r & 1u) * 16384u + wid * 2048u + l16;
    *(LAS f32x4*)rw = acc[r][0][1] + ac2[0][1]; *(LAS f32x4*)(rw + 1024u) = acc[r][1][1] + ac2[1][1]; prev[0] = acc[r][0][0] + ac2[0][0]; prev[1] = acc[r][1][0] + ac2[1][0];
    if (r == 0) WAITV(0); else if (r < 3) WAITV(2);
    BAR();
  };
#define UC(n) std::integral_constant<unsigned, n>{}
  rowblk(UC(0)); rowblk(UC(1)); rowblk(UC(2)); rowblk(UC(3));
  {
    f32x4 a0 = prev[0] + *(LAS f32x4*)(red + 16384u + (wid ^ 4u) * 2048u + l16), a1 = prev[1] + *(LAS f32x4*)(red + 16384u + (wid ^ 4u) * 2048u + 1024u + l16);
    for (unsigned sp = 0; sp < SPIN_CAP; ++sp) {
      bool ok = true;
#pragma unroll
      for (int jj = 0; jj < 6; ++jj) ok &= sl[jj >> 1][jj & 1][3] == t32;
      if (__all(ok)) break;
      asm volatile("" ::: "memory");
      SLD(0, 6);
    }
#pragma unroll
    for (int i = 0; i < 3; ++i) { a0 += unpack24(sl[i][0]); a1 += unpack24(sl[i][1]); }
    char* op = out + (size_t)(b * 128u + kq * 32u + c) * 2048u + nl * 256u + w4 * 64u + grp * 32u + g * 8u;
    *(u32x2*)op = u32x2{cvtpk(a0[0], a0[1]), cvtpk(a0[2], a0[3])};
    *(u32x2*)(op + 32768) = u32x2{cvtpk(a1[0], a1[1]), cvtpk(a1[2], a1[3])};
    if (h == 0 && tid == 0) st_flag(ws + WS_EPOCH + b * 128u, tag);
  }
  asm volatile(".Lmha_end:");
}

extern "C" void kernel_launch(void* const* d_in, const int* in_sizes, int n_in, void* d_out, int out_size, void* d_ws, size_t ws_size, hipStream_t stream) {
  (void)in_sizes; (void)n_in; (void)out_size; (void)ws_size;
  (void)hipFuncSetAttribute((const void*)mha_fused, hipFuncAttributeMaxDynamicSharedMemorySize, 163840);
  mha_fused<<<256, 512, 163840, stream>>>((const char*)d_in[0], (const char*)d_in[1], (const char*)d_in[2], (const char*)d_in[3], (const char*)d_in[4], (char*)d_out, (char*)d_ws);
}
```
